```python
import torch

def _softmax(x, axis=-1):
    return torch.softmax(x, dim=axis)
def _gelu(x):
    return torch.nn.functional.gelu(x, approximate="tanh")
def _silu(x):
    return torch.nn.functional.silu(x)
def _sigmoid(x):
    return torch.sigmoid(x)
def _layernorm(x, axis=-1, eps=1e-5):
    m = x.mean(dim=axis, keepdim=True)
    v = x.var(dim=axis, keepdim=True, unbiased=False)
    return (x - m) / torch.sqrt(v + eps)
def _scatter_add(tgt, idx, src, axis=0):
    return tgt.clone().index_add_(axis, idx, src)
def _segment_sum(src, seg, n):
    out = torch.zeros((n,) + src.shape[1:], dtype=src.dtype)
    return out.index_add_(0, seg, src)
def _pad(x, pw):
    flat = tuple(v for pair in reversed(pw) for v in pair)
    return torch.nn.functional.pad(x, flat)

kernel_name = 'gated_mix__softmax_full_mha__ssm_conv_h3__prompt'

def setup_inputs():

    _w = (lambda a: (lambda t: t.to(torch.bfloat16) if t.is_floating_point() else t)(torch.as_tensor(a)))
    def _A_setup():
        B, S, D = 1, 2048, 640
        return {
            "x": _w((lambda *s: torch.randn(*s, dtype=torch.bfloat16) * 0.05)(B, S, D)),
            "wqkv": _w((lambda *s: torch.randn(*s, dtype=torch.bfloat16) * 0.8)(D, 3 * D)),
            "wo": _w((lambda *s: torch.randn(*s, dtype=torch.bfloat16) * 0.05)(D, D)),
        }
    _A_in = _A_setup()

    def _B_setup():
        B, S, D = 1, 2048, 640
        ks = 4
        return {
            "q": _w((lambda *s: torch.randn(*s, dtype=torch.bfloat16) * 0.05)(B, S, D)),
            "k": _w((lambda *s: torch.randn(*s, dtype=torch.bfloat16) * 0.8)(B, S, D)),
            "v": _w((lambda *s: torch.randn(*s, dtype=torch.bfloat16) * 0.4)(B, S, D)),
            "shift_k": _w((lambda *s: torch.randn(*s, dtype=torch.bfloat16) * 0.8)(D, ks) * 0.5),
            "ssm_k": _w((lambda *s: torch.randn(*s, dtype=torch.bfloat16) * 0.8)(D, S) * (1.0 / S ** 0.5)),
            "ssm_D": _w((lambda *s: torch.randn(*s, dtype=torch.bfloat16) * 0.4)(D)),
        }
    _B_in = _B_setup()

    return {
        "A_x": _A_in["x"],
        "A_wqkv": _A_in["wqkv"],
        "A_wo": _A_in["wo"],
        "B_q": _B_in["q"],
        "B_k": _B_in["k"],
        "B_v": _B_in["v"],
        "B_shift_k": _B_in["shift_k"],
        "B_ssm_k": _B_in["ssm_k"],
        "B_ssm_D": _B_in["ssm_D"],
        "gate_w": _w((lambda *s: torch.randn(*s, dtype=torch.bfloat16) * 0.05)(640) * 16.0),
    }

def reference(A_x, A_wqkv, A_wo, B_q, B_k, B_v, B_shift_k, B_ssm_k, B_ssm_D, gate_w):
    def _A_ref(x, wqkv, wo):
        B, S, D = x.shape
        H = 32; Dh = D // H
        qkv = (lambda x, *d: x.permute(*d))((x @ wqkv).reshape(B, S, 3, H, Dh), 2, 0, 3, 1, 4)
        q, k, v = qkv[0], qkv[1], qkv[2]
        scale = Dh ** -0.5
        scores = torch.einsum("bhqd,bhkd->bhqk", q, k) * scale
        p = _softmax(scores, axis=-1)
        o = torch.einsum("bhqk,bhkd->bhqd", p, v)
        return (lambda x, *d: x.permute(*d))(o, 0, 2, 1, 3).reshape(B, S, D) @ wo

    def _B_ref(q, k, v, shift_k, ssm_k, ssm_D):
        B, S, D = q.shape
        ks = shift_k.shape[-1]
        k_pad = _pad(k, ((0, 0), (ks - 1, 0), (0, 0)))
        k_shift = torch.zeros_like(k)
        for t in range(ks):
            k_shift = k_shift + k_pad[:, t:t + S, :] * shift_k[:, ks - 1 - t][None, None, :]
        kv = k_shift * v
        L = 2 * S
        kv_pad = _pad(kv, ((0, 0), (0, S), (0, 0)))
        kf_pad = _pad(ssm_k, ((0, 0), (0, S)))
        kv_f = (lambda x, axes=(-2,-1): torch.fft.rfft2(x.float(), dim=axes))(kv_pad[:, None, :, :], axes=(1, 2))
        kf_f = (lambda x, axes=(-2,-1): torch.fft.rfft2(x.float(), dim=axes))(kf_pad[:, None, :], axes=(1, 2))
        kf_f = (lambda x, *d: x.permute(*d))(kf_f, 1, 2, 0)
        y_f = kv_f * kf_f[None, :, :, :]
        y = (lambda x, s, axes=(-2,-1): torch.fft.irfft2(x, s=s, dim=axes).to(torch.bfloat16))(y_f, (1, L), axes=(1, 2))[:, 0, :S, :]
        y = y + kv * ssm_D[None, None, :]
        return q * y

    x = A_x
    g = _sigmoid((x * gate_w).sum(-1, keepdims=True))
    a = _A_ref(x, A_wqkv, A_wo)
    b = _B_ref(x, B_k, B_v, B_shift_k, B_ssm_k, B_ssm_D)
    return g * a + (1 - g) * b
```

```cpp
#include <hip/hip_runtime.h>
#include <hip/hip_bf16.h>
#include <stdint.h>

typedef __attribute__((ext_vector_type(8))) __bf16 bf16x8;
typedef __attribute__((ext_vector_type(4))) __bf16 bf16x4;
typedef __attribute__((ext_vector_type(2))) __bf16 bf16x2;
typedef __attribute__((ext_vector_type(4))) short s16x4;
typedef __attribute__((ext_vector_type(16))) float f32x16;
typedef __attribute__((ext_vector_type(4))) float f32x4;
typedef __attribute__((ext_vector_type(2))) float f32x2;
typedef __attribute__((ext_vector_type(4))) unsigned u32x4;
typedef __attribute__((ext_vector_type(2))) unsigned u32x2;
typedef __attribute__((address_space(3))) void* lptr;
typedef const __attribute__((address_space(1))) void* gptr;
typedef __attribute__((address_space(3))) s16x4* lptr_s16x4;

#define DEV __device__ __forceinline__

constexpr int SEQ = 2048, DM = 640;
constexpr size_t WS_QK = 0;
constexpr size_t WS_V = WS_QK + (size_t)160 * 2048 * 16;
constexpr size_t WS_UT = WS_V + (size_t)32 * 64 * 1344;
constexpr size_t WS_ZT = WS_UT + (size_t)640 * 4096 + 256;
constexpr size_t WS_O = WS_ZT + (size_t)640 * 4096;
constexpr size_t WS_G = WS_O + (size_t)80 * 2048 * 16;

DEV unsigned pk_bf16(float lo, float hi) {
  f32x2 v = {lo, hi};
  bf16x2 b = __builtin_convertvector(v, bf16x2);
  return __builtin_bit_cast(unsigned, b);
}
DEV float bf_lo(unsigned u) { return __builtin_bit_cast(float, u << 16); }
DEV float bf_hi(unsigned u) { return __builtin_bit_cast(float, u & 0xffff0000u); }
DEV u32x2 tr_read(const char* p) {
  s16x4 v = __builtin_amdgcn_ds_read_tr16_b64_v4i16((lptr_s16x4)p);
  return __builtin_bit_cast(u32x2, v);
}
DEV void glds16(const void* g, char* l) { __builtin_amdgcn_global_load_lds((gptr)g, (lptr)l, 16, 0, 0); }
DEV f32x16 mfma32(u32x4 a, u32x4 b, f32x16 c) {
  return __builtin_amdgcn_mfma_f32_32x32x16_bf16(__builtin_bit_cast(bf16x8, a), __builtin_bit_cast(bf16x8, b), c, 0, 0, 0);
}
DEV f32x16 mfma32l(u32x2 a, u32x2 b, f32x16 c) {
  return __builtin_amdgcn_mfma_f32_32x32x8bf16_1k(__builtin_bit_cast(s16x4, a), __builtin_bit_cast(s16x4, b), c, 0, 0, 0);
}
DEV f32x4 mfma16(u32x4 a, u32x4 b, f32x4 c) {
  return __builtin_amdgcn_mfma_f32_16x16x32_bf16(__builtin_bit_cast(bf16x8, a), __builtin_bit_cast(bf16x8, b), c, 0, 0, 0);
}
#define WAIT_VM(n) asm volatile("s_waitcnt vmcnt(" #n ")" ::: "memory")
#define RAW_BARRIER() do { asm volatile("s_waitcnt lgkmcnt(0)" ::: "memory"); __builtin_amdgcn_s_barrier(); asm volatile("" ::: "memory"); } while (0)

constexpr int K1_STAGE = 32768, K1_KV = 4 * K1_STAGE;
constexpr int K1_LDS = K1_KV + 9216 + 8192 + 1024;

DEV void kv_issue(const char* bk, const char* bv, const char* bsk, char* smem, int tile, int wave, int lane) {
  const int tb = tile / 10, chb = tile - tb * 10;
  const int t0 = tb * 64, rr = lane >> 3, cc = lane & 7;
  char* kb = smem + K1_KV;
  glds16(bk + (size_t)(t0 + wave * 8 + rr) * 1280 + chb * 128 + cc * 16, kb + 1024 + wave * 1024);
  glds16(bv + (size_t)(t0 + wave * 8 + rr) * 1280 + chb * 128 + cc * 16, kb + 9216 + wave * 1024);
  if (wave == 0) {
    int row = t0 - 8 + rr; row = row < 0 ? 0 : row;
    glds16(bk + (size_t)row * 1280 + chb * 128 + cc * 16, kb);
  } else if (wave == 1) {
    glds16(bsk + chb * 512 + (lane & 31) * 16, kb + 9216 + 8192);
  }
}

DEV void kv_compute(char* ut, const char* smem, int tile, int wave, int lane) {
  const int tb = tile / 10, chb = tile - tb * 10;
  const char* kb = smem + K1_KV;
  const int cg = wave & 3, oct = (wave >> 2) * 4 + (lane >> 4);
  const int q = (lane & 15) >> 2, p = lane & 3;
  const char* ka = kb + (oct * 8 + 4 + q) * 128 + cg * 32 + p * 8;
  u32x2 k0 = tr_read(ka), k1 = tr_read(ka + 512), k2 = tr_read(ka + 1024);
  const char* va = kb + 9216 + (oct * 8 + q) * 128 + cg * 32 + p * 8;
  u32x2 v0 = tr_read(va), v1 = tr_read(va + 512);
  u32x2 tp = *(const u32x2*)(kb + 9216 + 8192 + (cg * 16 + (lane & 15)) * 8);
  if (tb == 0 && oct == 0) { k0[0] = 0; k0[1] = 0; }
  float kk[12] = {bf_lo(k0[0]), bf_hi(k0[0]), bf_lo(k0[1]), bf_hi(k0[1]), bf_lo(k1[0]), bf_hi(k1[0]), bf_lo(k1[1]), bf_hi(k1[1]),
                  bf_lo(k2[0]), bf_hi(k2[0]), bf_lo(k2[1]), bf_hi(k2[1])};
  float vv[8] = {bf_lo(v0[0]), bf_hi(v0[0]), bf_lo(v0[1]), bf_hi(v0[1]), bf_lo(v1[0]), bf_hi(v1[0]), bf_lo(v1[1]), bf_hi(v1[1])};
  const float w0 = bf_lo(tp[0]), w1 = bf_hi(tp[0]), w2 = bf_lo(tp[1]), w3 = bf_hi(tp[1]);
  float u[8];
#pragma unroll
  for (int i = 0; i < 8; ++i) u[i] = (kk[i + 4] * w0 + kk[i + 3] * w1 + kk[i + 2] * w2 + kk[i + 1] * w3) * vv[i];
  u32x4 o = {pk_bf16(u[7], u[6]), pk_bf16(u[5], u[4]), pk_bf16(u[3], u[2]), pk_bf16(u[1], u[0])};
  const int ch = chb * 64 + cg * 16 + (lane & 15);
  const int x0 = 2047 - (tb * 64 + oct * 8 + 7);
  *(u32x4*)(ut + (size_t)ch * 4096 + x0 * 2) = o;
}

template <bool VT>
DEV void k1_gemm(const char* xs, const char* wsrc, char* smem, char* ws, int rb, int cb, int rot, int wave, int lane, int tile) {
  const int r = lane & 31, hh = lane >> 5;
  const int wt = wave & 1, wc = wave >> 1;
  auto issue = [&](int it) {
    int s = it + rot; s = s >= 10 ? s - 10 : s;
    char* st = smem + (it & 3) * K1_STAGE;
    glds16(xs + s * 128, st + wave * 1024);
    glds16(xs + s * 128 + 64 * 1280, st + 8192 + wave * 1024);
    glds16(wsrc + (size_t)s * 64 * 3840, st + 16384 + wave * 1024);
    glds16(wsrc + (size_t)s * 64 * 3840 + 32 * 3840, st + 16384 + 8192 + wave * 1024);
  };
  issue(0); issue(1); issue(2);
  int xa[4];
#pragma unroll
  for (int ks = 0; ks < 4; ++ks) xa[ks] = (wt * 64 + r) * 128 + (((2 * ks + hh) ^ ((r >> 1) & 7)) << 4);
  int wa[2];
  {
    const int blk = (lane >> 4) & 1, q = (lane & 15) >> 2, p = lane & 3;
    const int ch = 4 * wc + 2 * blk + (VT ? (p >> 1) : (p & 1)), half = VT ? (p & 1) : (p >> 1);
#pragma unroll
    for (int t = 0; t < 2; ++t) wa[t] = 16384 + (8 * hh + 4 * t + q) * 256 + ((ch ^ ((q << 2) | (2 * hh + t))) << 4) + 8 * half;
  }
  f32x16 acc[2] = {};
  WAIT_VM(12);
  RAW_BARRIER();
  kv_compute(ws + WS_UT, smem, tile, wave, lane);
#pragma unroll
  for (int it = 0; it < 10; ++it) {
    if (it < 8) WAIT_VM(8); else if (it == 8) WAIT_VM(4); else WAIT_VM(0);
    RAW_BARRIER();
    if (it + 3 < 10) issue(it + 3);
    const char* st = smem + (it & 3) * K1_STAGE;
#pragma unroll
    for (int ks = 0; ks < 4; ++ks) {
      u32x4 x0 = *(const u32x4*)(st + xa[ks]);
      u32x4 x1 = *(const u32x4*)(st + xa[ks] + 4096);
      u32x2 w0 = tr_read(st + wa[0] + ks * 4096), w1 = tr_read(st + wa[1] + ks * 4096);
      u32x4 w = {w0[0], w0[1], w1[0], w1[1]};
      if (VT) { acc[0] = mfma32(x0, w, acc[0]); acc[1] = mfma32(x1, w, acc[1]); }
      else { acc[0] = mfma32(w, x0, acc[0]); acc[1] = mfma32(w, x1, acc[1]); }
    }
  }
  if (!VT) {
    const float sc = cb < 5 ? 0.22360679774997896f * 1.4426950408889634f : 1.0f;
#pragma unroll
    for (int i = 0; i < 2; ++i) {
      const int t = rb * 128 + wt * 64 + i * 32 + r;
#pragma unroll
      for (int s = 0; s < 2; ++s) {
        u32x4 o;
#pragma unroll
        for (int j = 0; j < 4; ++j) o[j] = pk_bf16(acc[i][8 * s + 2 * j] * sc, acc[i][8 * s + 2 * j + 1] * sc);
        const int chunk = cb * 16 + wc * 4 + 2 * s + hh;
        *(u32x4*)(ws + WS_QK + ((size_t)chunk * 2048 + t) * 16) = o;
      }
    }
  } else {
    const int c = (cb - 10) * 128 + wc * 32 + r;
    const int head = c / 20, d = c - head * 20;
#pragma unroll
    for (int i = 0; i < 2; ++i) {
      const int kt = rb * 4 + wt * 2 + i;
#pragma unroll
      for (int s = 0; s < 2; ++s) {
        u32x4 o;
#pragma unroll
        for (int j = 0; j < 4; ++j) o[j] = pk_bf16(acc[i][8 * s + 2 * j], acc[i][8 * s + 2 * j + 1]);
        char* dst = ws + WS_V + (size_t)(((head * 64 + kt) * 2 + s) * 2 + hh) * 336 + d * 16;
        *(u32x4*)dst = o;
        if (d == 0) { u32x4 one = {0x3f803f80u, 0x3f803f80u, 0x3f803f80u, 0x3f803f80u}; *(u32x4*)(dst + 320) = one; }
      }
    }
  }
}

__global__ __launch_bounds__(512, 2) void k1_qkv(const char* __restrict__ x, const char* __restrict__ wqkv, const char* __restrict__ bk,
                                                 const char* __restrict__ bv, const char* __restrict__ bsk, char* __restrict__ ws) {
  __shared__ __attribute__((aligned(16))) char smem[K1_LDS];
  const int tid = threadIdx.x, lane = tid & 63, wave = __builtin_amdgcn_readfirstlane(tid >> 6);
  const int b = blockIdx.x, xcd = b & 7, rr = b >> 3;
  const int rb = (xcd >> 1) * 4 + (rr & 3), cb = (xcd & 1) * 8 + (rr >> 2);
  if (cb == 15) {
    for (int i = 0; i < 5; ++i) {
      const int tile = 240 + rb * 5 + i;
      kv_issue(bk, bv, bsk, smem, tile, wave, lane);
      WAIT_VM(0);
      RAW_BARRIER();
      kv_compute(ws + WS_UT, smem, tile, wave, lane);
      RAW_BARRIER();
    }
    return;
  }
  const int tile = rb * 15 + cb;
  kv_issue(bk, bv, bsk, smem, tile, wave, lane);
  const int xrow = wave * 8 + (lane >> 3);
  const char* xs = x + (size_t)(rb * 128 + xrow) * 1280 + (((lane & 7) ^ ((xrow >> 1) & 7)) << 4);
  const int wrow = wave * 4 + (lane >> 4);
  const char* wsrc = wqkv + (size_t)wrow * 3840 + cb * 256 + (((lane & 15) ^ (((wrow & 3) << 2) | ((wrow >> 2) & 3))) << 4);
  const int rot = (cb + 3 * (rb & 3) + (rb >> 2)) % 10;
  if (cb < 10) k1_gemm<false>(xs, wsrc, smem, ws, rb, cb, rot, wave, lane, tile);
  else k1_gemm<true>(xs, wsrc, smem, ws, rb, cb, rot, wave, lane, tile);
}

constexpr int CV_H = 4288, CV_U = 6784, CV_CH = 4 * CV_H + 2 * CV_U;
constexpr int K2_RED = 3 * CV_CH;
constexpr int K2_CRED = K2_RED + 8 * 4096;
constexpr int K2_LDS = K2_CRED + 3 * 4096;

typedef u32x4 __attribute__((aligned(2))) u32x4_u;

struct KVFrag { u32x4 k1[2]; u32x2 k2; u32x4 v[2][2]; };

DEV void load_kv(KVFrag& f, const char* k1p, const char* k2p, const char* vp, int it) {
  f.k1[0] = *(const u32x4*)(k1p + it * 1024);
  f.k1[1] = *(const u32x4*)(k1p + it * 1024 + 512);
  f.k2 = *(const u32x2*)(k2p + it * 1024);
  f.v[0][0] = *(const u32x4*)(vp + it * 2688);
  f.v[0][1] = *(const u32x4*)(vp + it * 2688 + 672);
  f.v[1][0] = *(const u32x4*)(vp + it * 2688 + 1344);
  f.v[1][1] = *(const u32x4*)(vp + it * 2688 + 2016);
}

DEV void attn_tile(const u32x4& k1, const u32x2& k2, const u32x4& va, const u32x4& vb, const u32x4& q1, const u32x2& q2, f32x16& o) {
  f32x16 z = {};
  f32x16 s = mfma32l(k2, q2, z);
  s = mfma32(k1, q1, s);
  u32x4 pa, pb;
#pragma unroll
  for (int j = 0; j < 4; ++j) pa[j] = pk_bf16(__builtin_amdgcn_exp2f(s[2 * j]), __builtin_amdgcn_exp2f(s[2 * j + 1]));
#pragma unroll
  for (int j = 0; j < 4; ++j) pb[j] = pk_bf16(__builtin_amdgcn_exp2f(s[8 + 2 * j]), __builtin_amdgcn_exp2f(s[8 + 2 * j + 1]));
  o = mfma32(va, pa, o);
  o = mfma32(vb, pb, o);
}

DEV void attn_safe(const char* k1p, const char* k2p, const char* vp, u32x4 q1, u32x2 q2a, u32x2 q2b, f32x16& o_out) {
  f32x16 o = {};
  float m = -INFINITY;
#pragma unroll 1
  for (int it = 0; it < 32; ++it) {
    KVFrag f;
    load_kv(f, k1p, k2p, vp, it);
#pragma unroll
    for (int kt = 0; kt < 2; ++kt) {
      f32x16 z = {};
      f32x16 s = mfma32l(f.k2, kt ? q2b : q2a, z);
      s = mfma32(f.k1[kt], q1, s);
      float tm = s[0];
#pragma unroll
      for (int j = 1; j < 16; ++j) tm = fmaxf(tm, s[j]);
      tm = fmaxf(tm, __shfl_xor(tm, 32));
      const float mn = fmaxf(m, tm);
      const float al = __builtin_amdgcn_exp2f(m - mn);
      m = mn;
#pragma unroll
      for (int j = 0; j < 16; ++j) o[j] *= al;
      u32x4 pa, pb;
#pragma unroll
      for (int j = 0; j < 4; ++j) pa[j] = pk_bf16(__builtin_amdgcn_exp2f(s[2 * j] - m), __builtin_amdgcn_exp2f(s[2 * j + 1] - m));
#pragma unroll
      for (int j = 0; j < 4; ++j) pb[j] = pk_bf16(__builtin_amdgcn_exp2f(s[8 + 2 * j] - m), __builtin_amdgcn_exp2f(s[8 + 2 * j + 1] - m));
      o = mfma32(f.v[kt][0], pa, o);
      o = mfma32(f.v[kt][1], pb, o);
    }
  }
  o_out = o;
}

__global__ __launch_bounds__(512, 2) void k2_attn(const char* __restrict__ x, const char* __restrict__ ssm_k, const char* __restrict__ ssm_D,
                                                  const char* __restrict__ gate_w, char* __restrict__ ws) {
  __shared__ __attribute__((aligned(16))) char smem[K2_LDS];
  const int tid = threadIdx.x, lane = tid & 63, wave = __builtin_amdgcn_readfirstlane(tid >> 6);
  const int b = blockIdx.x, xcd = b & 7, rr = b >> 3;
  const int head = xcd * 4 + (rr & 3), qblk = rr >> 2;
  const int r = lane & 31, hh = lane >> 5;
  const int wg = head * 8 + qblk;

  const int pair = wg >> 1, odd = wg & 1;
  {
#pragma unroll
    for (int ci = 0; ci < 3; ++ci) {
      const int ch = pair * 5 + (ci < 2 ? odd * 2 + ci : 4);
      char* base = smem + ci * CV_CH;
      const char* hrow = ssm_k + (size_t)ch * 4096;
      const char* urow = ws + WS_UT + (size_t)ch * 4096;
      {
        const int L = tid & 255, qn = 8 + (L < 254 ? L : 254);
#pragma unroll
        for (int e = 0; e < 2; ++e) {
          const int dl = (tid >> 8) + 2 * e;
          u32x4 v = *(const u32x4_u*)(hrow + (8 * qn + dl - 59) * 2);
          *(u32x4*)(base + dl * CV_H + qn * 16) = v;
        }
        if (tid < 256) {
          const int dl = tid >> 6, i = tid & 63, idx = i + dl - 59;
          unsigned short hv = 0;
          if (idx >= 0) hv = *(const unsigned short*)(hrow + idx * 2);
          *(unsigned short*)(base + dl * CV_H + i * 2) = hv;
        }
      }
      {
        const int L = tid & 255, pi = tid >> 8;
        u32x4 v = *(const u32x4_u*)(urow + L * 16 + (pi ? 0 : 8));
        if (pi == 0 && L == 255) { v[2] = 0; v[3] = 0; }
        char* ub = base + 4 * CV_H + pi * CV_U;
        *(u32x4*)(ub + (L >> 3) * 144 + (L & 7) * 16) = v;
        if (L < 120) { u32x4 z = {0, 0, 0, 0}; *(u32x4*)(ub + (32 + (L >> 3)) * 144 + (L & 7) * 16) = z; }
      }
    }
  }
  {
    const int row = wg * 8 + wave;
    float acc = 0.f;
#pragma unroll
    for (int i = 0; i < 5; ++i) {
      unsigned xv = *(const unsigned*)(x + (size_t)row * 1280 + i * 256 + lane * 4);
      unsigned gv = *(const unsigned*)(gate_w + i * 256 + lane * 4);
      acc += bf_lo(xv) * bf_lo(gv) + bf_hi(xv) * bf_hi(gv);
    }
#pragma unroll
    for (int o = 32; o >= 1; o >>= 1) acc += __shfl_xor(acc, o);
    if (lane == 0) *(float*)(ws + WS_G + row * 4) = 1.0f / (1.0f + __expf(-acc));
  }
  __syncthreads();
  int ci, unit;
  if (wave < 6) { ci = wave / 3; unit = wave - ci * 3; }
  else { ci = 2; unit = odd ? (wave == 6 ? 0 : -1) : (wave - 5); }
  f32x16 cacc = {};
  if (unit >= 0) {
    const char* base = smem + ci * CV_CH;
    const char* ha = base + (r & 3) * CV_H + ((r >> 2) + hh) * 16 + (unit == 2 ? 2048 : 0);
    const char* ua = base + 4 * CV_H + (r & 1) * CV_U + ((unit == 1 ? 15 : 31) - (r >> 1)) * 144 + hh * 16;
#pragma unroll
    for (int m = 0; m < 64; ++m) {
      u32x4 hf = *(const u32x4*)(ha + m * 32);
      u32x4 uf = *(const u32x4*)(ua + (m >> 2) * 144 + (m & 3) * 32);
      cacc = mfma32(hf, uf, cacc);
    }
    if (unit == 2) {
#pragma unroll
      for (int g = 0; g < 4; ++g) {
        f32x4 v = {cacc[4 * g], cacc[4 * g + 1], cacc[4 * g + 2], cacc[4 * g + 3]};
        *(f32x4*)(smem + K2_CRED + ci * 4096 + g * 1024 + lane * 16) = v;
      }
    }
  }
  __syncthreads();
  if (unit == 0 || unit == 1) {
    const int ch = pair * 5 + (ci < 2 ? odd * 2 + ci : 4);
    const float dv = bf_lo(*(const unsigned short*)(ssm_D + ch * 2));
    const char* ub = smem + ci * CV_CH + 4 * CV_H + CV_U;
#pragma unroll
    for (int g = 0; g < 4; ++g) {
      f32x4 v = {cacc[4 * g], cacc[4 * g + 1], cacc[4 * g + 2], cacc[4 * g + 3]};
      if (unit == 1) v += *(const f32x4*)(smem + K2_CRED + ci * 4096 + g * 1024 + lane * 16);
      const int t = unit * 1024 + 16 * g + 8 * hh + 4 * (r & 1) + 64 * (r >> 1);
      const int xx = 2044 - t;
      u32x2 uu = *(const u32x2*)(ub + (xx >> 6) * 144 + (xx & 63) * 2);
      u32x2 o = {pk_bf16(v[0] + dv * bf_hi(uu[1]), v[1] + dv * bf_lo(uu[1])), pk_bf16(v[2] + dv * bf_hi(uu[0]), v[3] + dv * bf_lo(uu[0]))};
      *(u32x2*)(ws + WS_ZT + (size_t)ch * 4096 + t * 2) = o;
    }
  }

  const int qb = wave & 3, kh = wave >> 2;
  const int hp = head >> 1, ho = head & 1;
  const int c1 = hp * 5 + (ho ? 3 : 0) + hh, c2 = hp * 5 + 2;
  const int q0 = qblk * 256 + qb * 64;
  const char* qk = ws + WS_QK;
  u32x4 q1[2]; u32x2 q2a[2], q2b[2];
#pragma unroll
  for (int i = 0; i < 2; ++i) {
    q1[i] = *(const u32x4*)(qk + ((size_t)c1 * 2048 + q0 + i * 32 + r) * 16);
    u32x2 t = *(const u32x2*)(qk + ((size_t)c2 * 2048 + q0 + i * 32 + r) * 16 + ho * 8);
    u32x2 z = {0, 0};
    q2a[i] = hh ? z : t;
    q2b[i] = hh ? t : z;
  }
  const char* k1p0 = qk + ((size_t)(80 + c1) * 2048 + r) * 16;
  const char* k2p0 = qk + ((size_t)(80 + c2) * 2048 + lane) * 16 + ho * 8;
  int dim;
  if (r < 16) dim = (r & 3) + 4 * ((r >> 3) & 1) + 8 * ((r >> 2) & 1) + (ho ? 4 : 0);
  else if (r < 20) dim = ho ? r - 16 : r;
  else dim = 20;
  const char* vp0 = ws + WS_V + (size_t)head * 64 * 1344 + hh * 336 + dim * 16;
  const char* k1p = k1p0 + kh * 16 * 1024;
  const char* k2p = k2p0 + kh * 16 * 1024;
  const char* vp = vp0 + kh * 16 * 2688;
  f32x16 o[2] = {};
  KVFrag fa, fb;
  load_kv(fa, k1p, k2p, vp, 0);
#pragma unroll 1
  for (int it = 0; it < 16; it += 2) {
    load_kv(fb, k1p, k2p, vp, it + 1);
    attn_tile(fa.k1[0], fa.k2, fa.v[0][0], fa.v[0][1], q1[0], q2a[0], o[0]);
    attn_tile(fa.k1[0], fa.k2, fa.v[0][0], fa.v[0][1], q1[1], q2a[1], o[1]);
    attn_tile(fa.k1[1], fa.k2, fa.v[1][0], fa.v[1][1], q1[0], q2b[0], o[0]);
    attn_tile(fa.k1[1], fa.k2, fa.v[1][0], fa.v[1][1], q1[1], q2b[1], o[1]);
    load_kv(fa, k1p, k2p, vp, it + 2 < 16 ? it + 2 : 15);
    attn_tile(fb.k1[0], fb.k2, fb.v[0][0], fb.v[0][1], q1[0], q2a[0], o[0]);
    attn_tile(fb.k1[0], fb.k2, fb.v[0][0], fb.v[0][1], q1[1], q2a[1], o[1]);
    attn_tile(fb.k1[1], fb.k2, fb.v[1][0], fb.v[1][1], q1[0], q2b[0], o[0]);
    attn_tile(fb.k1[1], fb.k2, fb.v[1][0], fb.v[1][1], q1[1], q2b[1], o[1]);
  }
  {
    const f32x16 snd = kh ? o[0] : o[1];
#pragma unroll
    for (int g = 0; g < 4; ++g) {
      f32x4 v = {snd[4 * g], snd[4 * g + 1], snd[4 * g + 2], snd[4 * g + 3]};
      *(f32x4*)(smem + K2_RED + wave * 4096 + g * 1024 + lane * 16) = v;
    }
  }
  __syncthreads();
  f32x16 f = kh ? o[1] : o[0];
#pragma unroll
  for (int g = 0; g < 4; ++g) {
    f32x4 v = *(const f32x4*)(smem + K2_RED + (wave ^ 4) * 4096 + g * 1024 + lane * 16);
    f[4 * g] += v[0]; f[4 * g + 1] += v[1]; f[4 * g + 2] += v[2]; f[4 * g + 3] += v[3];
  }
  {
    float l = f[12];
    bool ok = (l > 0x1p-80f) && (l < 0x1p80f);
#pragma unroll
    for (int j = 0; j < 12; ++j) ok = ok && (fabsf(f[j]) < 0x1p126f);
    if (__any(!ok)) attn_safe(k1p0, k2p0, vp0, kh ? q1[1] : q1[0], kh ? q2a[1] : q2a[0], kh ? q2b[1] : q2b[0], f);
  }
  const float inv = 1.0f / f[12];
  const int t = q0 + kh * 32 + r;
  u32x4 o1 = {pk_bf16(f[0] * inv, f[1] * inv), pk_bf16(f[2] * inv, f[3] * inv), pk_bf16(f[4] * inv, f[5] * inv), pk_bf16(f[6] * inv, f[7] * inv)};
  *(u32x4*)(ws + WS_O + ((size_t)c1 * 2048 + t) * 16) = o1;
  if (hh == 0) {
    u32x2 o2 = {pk_bf16(f[8] * inv, f[9] * inv), pk_bf16(f[10] * inv, f[11] * inv)};
    *(u32x2*)(ws + WS_O + ((size_t)c2 * 2048 + t) * 16 + ho * 8) = o2;
  }
}

constexpr int K3_Z = 102400, K3_RED = K3_Z + 10240, K3_LDS = K3_RED + 8 * 3072;

__global__ __launch_bounds__(512, 2) void k3_out(const char* __restrict__ x, const char* __restrict__ wo, const char* __restrict__ ws, char* __restrict__ out) {
  __shared__ __attribute__((aligned(16))) char smem[K3_LDS];
  const int tid = threadIdx.x, lane = tid & 63, wave = __builtin_amdgcn_readfirstlane(tid >> 6);
  const int b = blockIdx.x, xcd = b & 7, rr = b >> 3;
  const int tb = (xcd >> 1) * 8 + (rr & 7), cb = (xcd & 1) * 4 + (rr >> 3);
  const int tb4 = wave & 3, kh = wave >> 2;
  const int r = lane & 15, g = lane >> 4, q = r >> 2, p = lane & 3;
#pragma unroll
  for (int i = 0; i < 13; ++i) {
    const int piece = i * 8 + wave;
    if (piece < 100) {
      const int n = piece * 64 + lane, rho = n / 10, cc = n - rho * 10;
      const int k = rho ^ (((rho >> 3) & 1) << 2);
      glds16(wo + (size_t)k * 1280 + cb * 160 + cc * 16, smem + piece * 1024);
    }
  }
  {
    glds16(ws + WS_ZT + (size_t)(cb * 80 + wave * 8 + (lane >> 3)) * 4096 + tb * 128 + (lane & 7) * 16, smem + K3_Z + wave * 1024);
    if (wave < 2) glds16(ws + WS_ZT + (size_t)(cb * 80 + 64 + wave * 8 + (lane >> 3)) * 4096 + tb * 128 + (lane & 7) * 16, smem + K3_Z + 8192 + wave * 1024);
  }
  const int t = tb * 64 + tb4 * 16 + r;
  u32x4 of[10];
#pragma unroll
  for (int ks = 0; ks < 10; ++ks) of[ks] = *(const u32x4*)(ws + WS_O + ((size_t)((kh * 10 + ks) * 4 + g) * 2048 + t) * 16);
  const float gt = *(const float*)(ws + WS_G + t * 4);
  u32x2 xv[3];
#pragma unroll
  for (int i = 0; i < 3; ++i) {
    const int n = kh ? (i < 2 ? 3 + i : 4) : i;
    xv[i] = *(const u32x2*)(x + (size_t)t * 1280 + cb * 160 + n * 32 + g * 8);
  }
  WAIT_VM(0);
  RAW_BARRIER();
  f32x4 acc[5] = {};
  const char* wa0 = smem + (kh * 320 + 8 * g + 4 * (g & 1) + q) * 160 + p * 8;
  const char* wa1 = smem + (kh * 320 + 8 * g + 4 * ((g & 1) ^ 1) + q) * 160 + p * 8;
#pragma unroll
  for (int ks = 0; ks < 10; ++ks) {
#pragma unroll
    for (int n = 0; n < 5; ++n) {
      u32x2 w0 = tr_read(wa0 + ks * 5120 + n * 32), w1 = tr_read(wa1 + ks * 5120 + n * 32);
      u32x4 w = {w0[0], w0[1], w1[0], w1[1]};
      acc[n] = mfma16(w, of[ks], acc[n]);
    }
  }
  char* red = smem + K3_RED + wave * 3072 + lane * 16;
  if (kh) { *(f32x4*)(red) = acc[0]; *(f32x4*)(red + 1024) = acc[1]; *(f32x4*)(red + 2048) = acc[2]; }
  else { *(f32x4*)(red) = acc[3]; *(f32x4*)(red + 1024) = acc[4]; }
  RAW_BARRIER();
  const char* rin = smem + K3_RED + (wave ^ 4) * 3072 + lane * 16;
#pragma unroll
  for (int i = 0; i < 3; ++i) {
    const int n = kh ? (i < 2 ? 3 + i : 4) : i;
    u32x2 zz = tr_read(smem + K3_Z + (n * 16 + 4 * g + q) * 128 + tb4 * 32 + p * 8);
    if (kh == 0 || i < 2) {
      f32x4 a = (kh ? acc[3 + (i < 2 ? i : 1)] : acc[i]) + *(const f32x4*)(rin + i * 1024);
      const float gi = 1.0f - gt;
      u32x2 o;
      o[0] = pk_bf16(gt * a[0] + gi * bf_lo(xv[i][0]) * bf_lo(zz[0]), gt * a[1] + gi * bf_hi(xv[i][0]) * bf_hi(zz[0]));
      o[1] = pk_bf16(gt * a[2] + gi * bf_lo(xv[i][1]) * bf_lo(zz[1]), gt * a[3] + gi * bf_hi(xv[i][1]) * bf_hi(zz[1]));
      *(u32x2*)(out + (size_t)t * 1280 + cb * 160 + n * 32 + g * 8) = o;
    }
  }
}

extern "C" void kernel_launch(void* const* d_in, const int* in_sizes, int n_in, void* d_out, int out_size, void* d_ws, size_t ws_size,
                              hipStream_t stream) {
  const char* x = (const char*)d_in[0];
  const char* wqkv = (const char*)d_in[1];
  const char* wo = (const char*)d_in[2];
  const char* bk = (const char*)d_in[4];
  const char* bv = (const char*)d_in[5];
  const char* bsk = (const char*)d_in[6];
  const char* ssm_k = (const char*)d_in[7];
  const char* ssm_D = (const char*)d_in[8];
  const char* gate_w = (const char*)d_in[9];
  char* ws = (char*)d_ws;
  k1_qkv<<<256, 512, 0, stream>>>(x, wqkv, bk, bv, bsk, ws);
  k2_attn<<<256, 512, 0, stream>>>(x, ssm_k, ssm_D, gate_w, ws);
  k3_out<<<256, 512, 0, stream>>>(x, wo, ws, (char*)d_out);
}
```

```cpp
#include <hip/hip_runtime.h>
#include <hip/hip_bf16.h>
#include <stdint.h>

typedef __attribute__((ext_vector_type(8))) __bf16 bf16x8;
typedef __attribute__((ext_vector_type(2))) __bf16 bf16x2;
typedef __attribute__((ext_vector_type(4))) short s16x4;
typedef __attribute__((ext_vector_type(16))) float f32x16;
typedef __attribute__((ext_vector_type(4))) float f32x4;
typedef __attribute__((ext_vector_type(2))) float f32x2;
typedef __attribute__((ext_vector_type(4))) unsigned u32x4;
typedef __attribute__((ext_vector_type(2))) unsigned u32x2;
typedef __attribute__((address_space(3))) void* lptr;
typedef __attribute__((address_space(3))) s16x4* lptr_s16x4;
typedef __attribute__((address_space(3))) unsigned* lptr_u32;
typedef const __attribute__((address_space(3))) u32x4* lptr_u32x4;
typedef unsigned long long u64;
typedef __attribute__((address_space(1))) u64 gu64;
typedef __amdgpu_buffer_rsrc_t rsrc_t;

#define DEV __device__ __forceinline__

constexpr unsigned WS_QK = 0;
constexpr unsigned WS_V = WS_QK + 160 * 2048 * 16;
constexpr unsigned WS_UT = WS_V + 33 * 64 * 1280;
constexpr unsigned WS_ZT = WS_UT + 640 * 4096 + 256;
constexpr unsigned WS_O = WS_ZT + 640 * 4096;
#define FUST 8
#define FST 128
constexpr unsigned WS_G = WS_O + 80 * 2048 * 16;
constexpr unsigned WS_FA = WS_G + 8192;
constexpr unsigned WS_FU = WS_FA + 1024 * FST;
constexpr unsigned WS_FZ = WS_FU + 256 * FUST;
constexpr unsigned WS_FO = WS_FZ + 256 * FUST;
constexpr unsigned WS_E = WS_FO + 256 * FUST + 2048;
constexpr unsigned WS_RANGE = 16u << 20;

constexpr int L_STAGE = 32768, L_KV = 4 * L_STAGE;
constexpr int CV_H = 4160, CV_U = 4576, CV_CH = 4 * CV_H + 2 * CV_U;
constexpr int L_CV = 65536, L_WO = 61184, L_Z = 36864, L_JUNK = 157184, L_SYNC = 163584, L_TOTAL = 163840;
enum { SY_A = 0, SY_S, SY_C0, SY_C1, SY_CS, SY_Z, SY_R0, SY_O0, SY_W };

#define TS(i)
#define TSA(i)
#define TS_DECL
#define TS_ARG
#define TS_PASS
#define TS_FLUSH()

#define TSB(i)
DEV unsigned pk_bf16(float lo, float hi) {
  f32x2 v = {lo, hi};
  bf16x2 b = __builtin_convertvector(v, bf16x2);
  return __builtin_bit_cast(unsigned, b);
}
DEV float bf_lo(unsigned u) { return __builtin_bit_cast(float, u << 16); }
DEV float bf_hi(unsigned u) { return __builtin_bit_cast(float, u & 0xffff0000u); }
DEV u32x2 tr_read(const char* p) {
  s16x4 v = __builtin_amdgcn_ds_read_tr16_b64_v4i16((lptr_s16x4)p);
  return __builtin_bit_cast(u32x2, v);
}
DEV u32x2 tr_read_a(unsigned a) { return __builtin_bit_cast(u32x2, __builtin_amdgcn_ds_read_tr16_b64_v4i16((lptr_s16x4)(size_t)a)); }
DEV unsigned lds_addr(const char* p) { return (unsigned)(size_t)(lptr)p; }
DEV void glds(const char* sbase, unsigned voff, unsigned la) {
  asm volatile("s_mov_b32 m0, %2\n\ts_nop 0\n\tglobal_load_lds_dwordx4 %0, %1" :: "v"(voff), "s"(sbase), "s"(la) : "memory");
}
DEV f32x16 mfma32(u32x4 a, u32x4 b, f32x16 c) {
  return __builtin_amdgcn_mfma_f32_32x32x16_bf16(__builtin_bit_cast(bf16x8, a), __builtin_bit_cast(bf16x8, b), c, 0, 0, 0);
}
DEV f32x16 mfma32l(u32x2 a, u32x2 b, f32x16 c) {
  return __builtin_amdgcn_mfma_f32_32x32x8bf16_1k(__builtin_bit_cast(s16x4, a), __builtin_bit_cast(s16x4, b), c, 0, 0, 0);
}
DEV f32x4 mfma16(u32x4 a, u32x4 b, f32x4 c) {
  return __builtin_amdgcn_mfma_f32_16x16x32_bf16(__builtin_bit_cast(bf16x8, a), __builtin_bit_cast(bf16x8, b), c, 0, 0, 0);
}
#define WAIT_VM(n) asm volatile("s_waitcnt vmcnt(" #n ")" ::: "memory")
#define RAW_BARRIER() do { asm volatile("s_waitcnt lgkmcnt(0)" ::: "memory"); __builtin_amdgcn_s_barrier(); asm volatile("" ::: "memory"); } while (0)
#define SCHED_FENCE() __builtin_amdgcn_sched_barrier(0)

DEV void st16(rsrc_t r, unsigned off, u32x4 v) { __builtin_amdgcn_raw_buffer_store_b128(v, r, off, 0, 16); }
DEV void st8(rsrc_t r, unsigned off, u32x2 v) { __builtin_amdgcn_raw_buffer_store_b64(v, r, off, 0, 16); }
DEV void st4(rsrc_t r, unsigned off, unsigned v) { __builtin_amdgcn_raw_buffer_store_b32(v, r, off, 0, 16); }
DEV u32x4 ld16(rsrc_t r, unsigned off) { return __builtin_amdgcn_raw_buffer_load_b128(r, off, 0, 16); }
DEV u32x2 ld8(rsrc_t r, unsigned off) { return __builtin_amdgcn_raw_buffer_load_b64(r, off, 0, 16); }
DEV unsigned ld4(rsrc_t r, unsigned off) { return __builtin_amdgcn_raw_buffer_load_b32(r, off, 0, 16); }

constexpr int SPIN_CAP = 40000;
#define FSLEEP 2
#define FDELAY 0
DEV void post_flag(char* ws, unsigned off, u64 tag) {
  __hip_atomic_store((gu64*)(ws + off), tag, __ATOMIC_RELAXED, __HIP_MEMORY_SCOPE_AGENT);
}
DEV void wait_flags(char* ws, unsigned off, u64 tag) {
  bool need = true;
#pragma unroll 1
  for (int i = 0; i < SPIN_CAP; ++i) {
    if (need) need = __hip_atomic_load((gu64*)(ws + off), __ATOMIC_RELAXED, __HIP_MEMORY_SCOPE_AGENT) != tag;
    if (!__any(need)) break;
    __builtin_amdgcn_s_sleep(FSLEEP);
  }
  __builtin_amdgcn_fence(__ATOMIC_ACQUIRE, "wavefront");
  asm volatile("" ::: "memory");
}
DEV unsigned lds_arrive(char* smem, int w, int lane) {
  unsigned r;
  const unsigned one = lane == 0;
  asm volatile("s_waitcnt lgkmcnt(0)\n\tds_add_rtn_u32 %0, %1, %2\n\ts_waitcnt lgkmcnt(0)"
               : "=&v"(r) : "v"(lds_addr(smem) + L_SYNC + (lane == 0 ? w : 16 + (lane & 31)) * 4), "v"(one) : "memory");
  return __builtin_amdgcn_readfirstlane(r);
}
DEV void lds_wait(char* smem, int w, unsigned n) {
#pragma unroll 1
  for (int i = 0; i < SPIN_CAP * 8; ++i) {
    const unsigned v = __hip_atomic_load((lptr_u32)(smem + L_SYNC + w * 4), __ATOMIC_RELAXED, __HIP_MEMORY_SCOPE_WORKGROUP);
    if (__builtin_amdgcn_readfirstlane(v) >= n) break;
    __builtin_amdgcn_s_sleep(1);
  }
  asm volatile("" ::: "memory");
}
DEV void drain_signal(char* smem, char* ws, int w, unsigned n, unsigned off, u64 tag, int lane) {
  WAIT_VM(0);
  if (lds_arrive(smem, w, lane) == n - 1 && lane == 0) post_flag(ws, off, tag);
}

DEV void kv_issue(const char* bk, const char* bv, const char* bsk, unsigned la, int wg, int kq, int lane) {
  const int t0 = (wg & 63) * 32, c0 = (wg >> 6) * 160;
  asm volatile("" : "+v"(lane));
  const char* src = ((kq & 2) ? bv : bk) + (t0 + (kq & 1) * 16) * 1280 + c0 * 2;
#pragma unroll
  for (int j = 0; j < 5; ++j) {
    const int n = j * 64 + lane, row = n / 20;
    glds(src, row * (1280 - 320) + n * 16, la + 2560 + kq * 5120 + j * 1024);
  }
  const int n = kq * 40 + lane, m = n - 80, mr = m / 20;
  int row = t0 - 4 + mr; row = row < 0 ? 0 : row;
  const char* p0 = m >= 0 ? bk + (row * 1280 + c0 * 2 + (m - mr * 20) * 16) : bsk + (c0 * 8 + n * 16);
  asm volatile("s_mov_b32 m0, %1\n\ts_nop 0\n\tglobal_load_lds_dwordx4 %0, off" :: "v"(p0), "s"(la + kq * 640) : "memory");
}
DEV void kv_math(unsigned la, rsrc_t srd, int wg, int kq, int lane) {
  const int t0 = (wg & 63) * 32, c0 = (wg >> 6) * 160;
  const int oct = lane >> 4, q = (lane & 15) >> 2, p = lane & 3;
  unsigned ka = la + 1280 + (oct * 8 + q) * 320 + p * 8 + kq * 32, ta = la + (kq * 16 + (lane & 15)) * 8;
  unsigned so = WS_UT + (c0 + kq * 16 + (lane & 15)) * 4096 + (2040 - t0 - oct * 8) * 2;
#pragma unroll 1
  for (int cg = kq; cg < 10; cg += 4) {
    u32x2 k0 = tr_read_a(ka);
    const u32x2 k1 = tr_read_a(ka + 1280), k2 = tr_read_a(ka + 2560);
    const u32x2 v0 = tr_read_a(ka + 11520), v1 = tr_read_a(ka + 12800);
    const u32x2 tp = *(const __attribute__((address_space(3))) u32x2*)(size_t)ta;
    if (t0 + oct == 0) { k0[0] = 0; k0[1] = 0; }
    const float kk[12] = {bf_lo(k0[0]), bf_hi(k0[0]), bf_lo(k0[1]), bf_hi(k0[1]), bf_lo(k1[0]), bf_hi(k1[0]),
                          bf_lo(k1[1]), bf_hi(k1[1]), bf_lo(k2[0]), bf_hi(k2[0]), bf_lo(k2[1]), bf_hi(k2[1])};
    const float vv[8] = {bf_lo(v0[0]), bf_hi(v0[0]), bf_lo(v0[1]), bf_hi(v0[1]), bf_lo(v1[0]), bf_hi(v1[0]), bf_lo(v1[1]), bf_hi(v1[1])};
    const float w0 = bf_lo(tp[0]), w1 = bf_hi(tp[0]), w2 = bf_lo(tp[1]), w3 = bf_hi(tp[1]);
    float u[8];
#pragma unroll
    for (int i = 0; i < 8; ++i) u[i] = (kk[i + 4] * w0 + kk[i + 3] * w1 + kk[i + 2] * w2 + kk[i + 1] * w3) * vv[i];
    const u32x4 o = {pk_bf16(u[7], u[6]), pk_bf16(u[5], u[4]), pk_bf16(u[3], u[2]), pk_bf16(u[1], u[0])};
    st16(srd, so, o);
    ka += 128; ta += 512; so += 64 * 4096;
  }
}
DEV void touch(const char* base, unsigned off, char* smem) {
  asm volatile("s_mov_b32 m0, %2\n\ts_nop 0\n\tglobal_load_lds_dword %0, %1" :: "v"(off), "s"(base), "s"(lds_addr(smem) + L_JUNK) : "memory");
}

struct Stage { u32x4 h[3]; u32x2 p[3]; u32x4 u[3]; u32x2 pf; };
DEV int cv_channel(int wg, int ci) { return (wg >> 1) * 5 + (ci < 2 ? (wg & 1) * 2 + ci : 4); }

DEV void h_issue(Stage& S, const char* ssm_k, int wg, int L) {
  const unsigned o0 = L * 16, o1 = o0 + 4096, o2 = L ? o0 - 8 : o0, o3 = o1 - 8;
  const char* r0 = ssm_k + cv_channel(wg, 0) * 4096;
  const char* r2 = ssm_k + cv_channel(wg, 2) * 4096;
  asm volatile(
      "global_load_dwordx4 %0, %6, %10\n\tglobal_load_dwordx4 %1, %7, %10\n\tglobal_load_dwordx4 %2, %6, %11\n\t"
      "global_load_dwordx2 %3, %8, %10\n\tglobal_load_dwordx2 %4, %9, %10\n\tglobal_load_dwordx2 %5, %8, %11"
      : "=&v"(S.h[0]), "=&v"(S.h[1]), "=&v"(S.h[2]), "=&v"(S.p[0]), "=&v"(S.p[1]), "=&v"(S.p[2])
      : "v"(o0), "v"(o1), "v"(o2), "v"(o3), "s"(r0), "s"(r2) : "memory");
}
DEV unsigned u_flag_off(int wg, int lane) {
  return WS_FU + ((wg & ~63) + lane) * FUST;
}
DEV void u_poll_issue(Stage& S, const char* ws, int wg, int lane) {
  asm volatile("global_load_dwordx2 %0, %1, %2 sc1" : "=&v"(S.pf) : "v"(u_flag_off(wg, lane)), "s"(ws) : "memory");
}
DEV void u_issue(Stage& S, char* ws, int wg, int L) {
  const unsigned o0 = WS_UT + cv_channel(wg, 0) * 4096 + L * 16, o1 = o0 + 4096, o2 = WS_UT + cv_channel(wg, 2) * 4096 + L * 16;
  asm volatile("global_load_dwordx4 %0, %3, %6 sc1\n\tglobal_load_dwordx4 %1, %4, %6 sc1\n\tglobal_load_dwordx4 %2, %5, %6 sc1"
               : "=&v"(S.u[0]), "=&v"(S.u[1]), "=&v"(S.u[2]) : "v"(o0), "v"(o1), "v"(o2), "s"(ws) : "memory");
}
DEV unsigned mid(unsigned hi, unsigned lo) { return __builtin_amdgcn_alignbit(hi, lo, 16); }
DEV void h_write(Stage& S, char* smem, int L) {
  asm volatile("" : "+v"(S.h[0]), "+v"(S.h[1]), "+v"(S.h[2]), "+v"(S.p[0]), "+v"(S.p[1]), "+v"(S.p[2]) :: "memory");
#pragma unroll
  for (int c = 0; c < 3; ++c) {
    const u32x4 w = S.h[c];
    const unsigned p0 = L ? S.p[c][0] : 0, p1 = L ? S.p[c][1] : 0;
    const unsigned m1 = mid(w[0], p1), m2 = mid(w[1], w[0]), m3 = mid(w[2], w[1]);
    char* dst = smem + L_CV + c * CV_CH + 48 + L * 16;
    const u32x4 c0 = {mid(p1, p0), m1, m2, m3}, c1 = {p1, w[0], w[1], w[2]}, c2 = {m1, m2, m3, mid(w[3], w[2])};
    *(u32x4*)dst = c0; *(u32x4*)(dst + CV_H) = c1; *(u32x4*)(dst + 2 * CV_H) = c2; *(u32x4*)(dst + 3 * CV_H) = w;
  }
  if (L < 204) {
    const int c = L / 68, i = L - c * 68;
    const int off = i < 12 ? (i / 3) * CV_H + (i % 3) * 16 : 4 * CV_H + ((i - 12) / 28) * CV_U + 4096 + ((i - 12) % 28) * 16;
    const u32x4 z = {0, 0, 0, 0};
    *(u32x4*)(smem + L_CV + c * CV_CH + off) = z;
  } else if (L < 207) {
    const u32x2 z = {0, 0};
    *(u32x2*)(smem + L_CV + (L - 204) * CV_CH + 4 * CV_H + 4088) = z;
  }
}
DEV void u_write(Stage& S, char* smem, int L) {
  asm volatile("" : "+v"(S.u[0]), "+v"(S.u[1]), "+v"(S.u[2]));
#pragma unroll
  for (int c = 0; c < 3; ++c) {
    char* d = smem + L_CV + c * CV_CH + 4 * CV_H + L * 16;
    *(u32x4*)(d + CV_U) = S.u[c];
    const u32x2 a = {S.u[c][0], S.u[c][1]}, b = {S.u[c][2], S.u[c][3]};
    *(u32x2*)(d - 8) = a; *(u32x2*)d = b;
  }
}

DEV u64 make_tag(const u32x2& ev) {
  unsigned lo, hi;
  asm volatile("v_readfirstlane_b32 %0, %2\n\tv_readfirstlane_b32 %1, %3\n\ts_nop 3" : "=&s"(lo), "=&s"(hi) : "v"(ev[0]), "v"(ev[1]));
  return (((u64)hi << 32) | lo) + 0x9E3779B97F4A7C15ull;
}

#define OUTAUX 16
#define POSTPRIO 2
#define YPRIO 1
#define WOTOUCH 2
#define BTPRIO 2
#define PSLEEP0 0
#define PSLEEP1 0
#define DEEP 0
#define OPRIO 0
#define EARLYPOLL 0
#define C1PRIO 2
#define C1WAIT 4
#define WOSLEEP 2
#define CVPRIO 3
#define ROT 1
#define ROT_EXPR (cb + 3 * (rb & 3) + (rb >> 2)) % 10
struct Dma { const char* x; const char* w; unsigned vx, vw, la; int s; };
DEV void dma_stage(Dma& D, int it) {
  const unsigned la = D.la + (it & 3) * L_STAGE;
  const char* px = D.x + D.s * 128;
  const char* pw = D.w + D.s * 64 * 3840;
#pragma unroll
  for (int j = 0; j < 4; ++j) {
    glds(px + j * 32 * 1280, D.vx, la + j * 4096);
    glds(pw + j * 16 * 3840, D.vw, la + 16384 + j * 4096);
  }
  D.s = D.s == 9 ? 0 : D.s + 1;
}

template <bool VT>
DEV void phase_a(char* smem, char* ws, rsrc_t srd, u64 tag, int rb, int cb, int wg, int wave, int lane TS_ARG) {
  const int r = lane & 31, hh = lane >> 5;
  const int wt = wave & 1, wc = wave >> 1;
  unsigned xa[4], wa[4];
#pragma unroll
  for (int ks = 0; ks < 4; ++ks) xa[ks] = lds_addr(smem) + (wt * 64 + r) * 128 + (((2 * ks + hh) ^ ((r >> 1) & 7)) << 4);
  {
    const int blk = (lane >> 4) & 1, q = (lane & 15) >> 2, p = lane & 3;
    const int ch = 8 * wc + 2 * blk + (VT ? (p >> 1) : (p & 1)), half = VT ? (p & 1) : (p >> 1);
#pragma unroll
    for (int t = 0; t < 4; ++t)
      wa[t] = lds_addr(smem) + 16384 + (8 * hh + 4 * (t & 1) + q) * 256 + (((ch + 4 * (t >> 1)) ^ ((q << 2) | (2 * hh + (t & 1)))) << 4) + 8 * half;
  }
  u32x4 fx[2][8], fw[2][8];
  auto rd = [&](int buf, int m) {
    const int ks = m >> 1, j = m & 1;
    fx[buf][m] = *(lptr_u32x4)(size_t)(xa[ks] + buf * L_STAGE + j * 4096);
    const u32x2 w0 = tr_read_a(wa[2 * j] + buf * L_STAGE + ks * 4096), w1 = tr_read_a(wa[2 * j + 1] + buf * L_STAGE + ks * 4096);
    const u32x4 w = {w0[0], w0[1], w1[0], w1[1]};
    fw[buf][m] = w;
  };
  f32x16 acc[4] = {};
  auto stage = [&](int buf, bool next) {
    SCHED_FENCE();
#pragma unroll
    for (int m = 0; m < 16; ++m) {
      if (next && m < 8) rd(buf ^ 1, m);
      const u32x4 xf = fx[buf][(m >> 2) * 2 + (m & 1)], wf = fw[buf][(m >> 2) * 2 + ((m >> 1) & 1)];
      acc[m & 3] = VT ? mfma32(xf, wf, acc[m & 3]) : mfma32(wf, xf, acc[m & 3]);
      SCHED_FENCE();
    }
  };
  RAW_BARRIER();
  TS(1);
#pragma unroll
  for (int m = 0; m < 8; ++m) rd(0, m);
  RAW_BARRIER();
  TS(2);
#pragma unroll 1
  for (int i = 0; i < 4; ++i) {
    stage(0, true);
#pragma unroll
    for (int k = 0; k < 4; ++k) { xa[k] ^= 2 * L_STAGE; wa[k] ^= 2 * L_STAGE; }
    WAIT_VM(0);
    RAW_BARRIER();
    if (i == 0 && wave == 0 && lane == 0) post_flag(ws, WS_FU + wg * FUST, tag);
    stage(1, true);
    RAW_BARRIER();
  }
  stage(0, true);
  stage(1, false);
  TS(3);
  if (!VT) {
    const float sc = cb < 5 ? 0.22360679774997896f * 1.4426950408889634f : 1.0f;
    const unsigned so = WS_QK + ((cb * 16 + wc * 8 + hh) * 2048 + rb * 128 + wt * 64 + r) * 16;
#pragma unroll
    for (int a = 0; a < 4; ++a)
#pragma unroll
      for (int s = 0; s < 2; ++s) {
        u32x4 o;
#pragma unroll
        for (int j = 0; j < 4; ++j) o[j] = pk_bf16(acc[a][8 * s + 2 * j] * sc, acc[a][8 * s + 2 * j + 1] * sc);
        st16(srd, so + (((a >> 1) * 4 + 2 * s) * 2048 + (a & 1) * 32) * 16, o);
      }
  } else {
#pragma unroll
    for (int jc = 0; jc < 2; ++jc) {
      const int c = (cb - 10) * 128 + wc * 64 + jc * 32 + r;
      const int head = c / 20, d = c - head * 20;
      const unsigned so = WS_V + (head * 64 + rb * 4 + wt * 2) * 1280 + (d >> 2) * 128 + hh * 64 + (d & 3) * 16;
#pragma unroll
      for (int i = 0; i < 2; ++i)
#pragma unroll
        for (int s = 0; s < 2; ++s) {
          u32x4 o;
#pragma unroll
          for (int j = 0; j < 4; ++j) o[j] = pk_bf16(acc[2 * jc + i][8 * s + 2 * j], acc[2 * jc + i][8 * s + 2 * j + 1]);
          st16(srd, so + (i * 2 + s) * 640, o);
        }
    }
  }
}

DEV float dot2(unsigned a, unsigned b, float c) {
  return __builtin_amdgcn_fdot2_f32_bf16(__builtin_bit_cast(bf16x2, a), __builtin_bit_cast(bf16x2, b), c, false);
}
DEV float row_sum16(float v) {
  v += __builtin_bit_cast(float, __builtin_amdgcn_update_dpp(0, __builtin_bit_cast(int, v), 0x128, 0xf, 0xf, false));
  v += __builtin_bit_cast(float, __builtin_amdgcn_update_dpp(0, __builtin_bit_cast(int, v), 0x124, 0xf, 0xf, false));
  v += __builtin_bit_cast(float, __builtin_amdgcn_update_dpp(0, __builtin_bit_cast(int, v), 0x122, 0xf, 0xf, false));
  v += __builtin_bit_cast(float, __builtin_amdgcn_update_dpp(0, __builtin_bit_cast(int, v), 0x121, 0xf, 0xf, false));
  return v;
}
DEV void gate_rows(const char* x, const char* gate_w, rsrc_t srd, int rb, int wave, int lane) {
  u32x4 gw[5], xv[8][5];
  const unsigned go = (lane & 15) * 16, xo = (rb * 128 + wave * 32 + (lane >> 4)) * 1280 + go;
#pragma unroll
  for (int i = 0; i < 5; ++i) gw[i] = *(const u32x4*)(gate_w + go + i * 256);
#pragma unroll
  for (int g = 0; g < 8; ++g)
#pragma unroll
    for (int i = 0; i < 5; ++i) xv[g][i] = *(const u32x4*)(x + xo + g * 5120 + i * 256);
  SCHED_FENCE();
  float gs = 0.f;
#pragma unroll
  for (int g = 0; g < 8; ++g) {
    float acc = 0.f;
#pragma unroll
    for (int i = 0; i < 5; ++i)
#pragma unroll
      for (int j = 0; j < 4; ++j) {
        const unsigned xe = xv[g][i][j], ge = gw[i][j];
        acc = dot2(xe, ge, acc);
      }
    acc = row_sum16(acc);
    if ((lane & 15) == g) gs = acc;
  }
  if ((lane & 15) < 8)
    st4(srd, WS_G + (rb * 128 + wave * 32 + (lane & 15) * 4 + (lane >> 4)) * 4, __builtin_bit_cast(unsigned, 1.0f / (1.0f + __expf(-gs))));
}

template <int MASK>
DEV void conv_run(const char* base, int s0, int s1, int lane, f32x4 (&acc)[8]) {
  constexpr int TOP = MASK >= 128 ? 8 : 6;
  const int i = lane & 15, g = lane >> 4;
  unsigned ha = lds_addr(base) + (i & 3) * CV_H + ((i >> 2) + g + 4 * s0) * 16;
  unsigned ua = lds_addr(base) + 4 * CV_H + (i & 1) * CV_U + (28 - 4 * (i >> 1) + g + 4 * s0) * 16;
  asm volatile("" : "+v"(ha), "+v"(ua));
  u32x4 A[8], B[8];
#pragma unroll
  for (int p = 0; p < TOP; ++p) {
    A[p] = *(lptr_u32x4)(size_t)(ha + p * 512);
    B[TOP - 1 - p] = *(lptr_u32x4)(size_t)(ua + (8 - TOP + p) * 512);
    SCHED_FENCE();
  }
#pragma unroll 1
  for (int s = s0; s < s1; ++s) {
    ha += 64; ua += 64;
    asm volatile("" : "+v"(ha), "+v"(ua));
#pragma unroll
    for (int p = 0; p < TOP; ++p) {
#pragma unroll
      for (int j = TOP - 1; j >= p; --j)
        if ((MASK >> j) & 1) acc[j] = mfma16(A[p], B[j - p], acc[j]);
      A[p] = *(lptr_u32x4)(size_t)(ha + p * 512);
      B[TOP - 1 - p] = *(lptr_u32x4)(size_t)(ua + (8 - TOP + p) * 512);
      SCHED_FENCE();
    }
  }
}
template <int MASK>
DEV void conv_send(char* red, int lane, const f32x4 (&acc)[8]) {
#pragma unroll
  for (int j = 0; j < 8; ++j)
    if ((MASK >> j) & 1) *(f32x4*)(red + j * 1024 + lane * 16) = acc[j];
}
template <int MASK>
DEV void conv_final(const char* red, const char* base, rsrc_t srd, float dv, int ch, int lane, const f32x4 (&acc)[8]) {
  const int c = lane & 15, g = lane >> 4;
  const int t0 = 8 * g + 4 * (c & 1) + 32 * (c >> 1);
#pragma unroll
  for (int j = 0; j < 8; ++j)
    if ((MASK >> j) & 1) {
      const f32x4 v = acc[j] + *(const f32x4*)(red + j * 1024 + lane * 16);
      const int t = 256 * j + t0;
      const u32x2 uu = *(const u32x2*)(base + 4 * CV_H + CV_U + (2044 - t) * 2);
      const u32x2 o = {pk_bf16(v[0] + dv * bf_hi(uu[1]), v[1] + dv * bf_lo(uu[1])), pk_bf16(v[2] + dv * bf_hi(uu[0]), v[3] + dv * bf_lo(uu[0]))};
      st8(srd, WS_ZT + ch * 4096 + t * 2, o);
    }
}

DEV void attn_safe(rsrc_t srd, unsigned vk1, unsigned vk2, unsigned vv, unsigned vq1, unsigned vq2, int hh, f32x16& o_out) {
  const u32x4 q1 = ld16(srd, vq1);
  const u32x2 qt = ld8(srd, vq2), zz = {0, 0};
  const u32x2 q2a = hh ? zz : qt, q2b = hh ? qt : zz;
  f32x16 o = {};
  float m = -INFINITY;
#pragma unroll 1
  for (int it = 0; it < 64; ++it) {
    const u32x4 k1 = ld16(srd, vk1 + it * 512);
    const u32x2 k2 = ld8(srd, vk2 + (it >> 1) * 1024);
    const u32x4 va = ld16(srd, vv + it * 1280), vb = ld16(srd, vv + it * 1280 + 640);
    f32x16 z = {};
    f32x16 s = mfma32l(k2, (it & 1) ? q2b : q2a, z);
    s = mfma32(k1, q1, s);
    float tm = s[0];
#pragma unroll
    for (int j = 1; j < 16; ++j) tm = fmaxf(tm, s[j]);
    tm = fmaxf(tm, __shfl_xor(tm, 32));
    const float mn = fmaxf(m, tm);
    const float al = __builtin_amdgcn_exp2f(m - mn);
    m = mn;
#pragma unroll
    for (int j = 0; j < 16; ++j) o[j] *= al;
    u32x4 pa, pb;
#pragma unroll
    for (int j = 0; j < 4; ++j) pa[j] = pk_bf16(__builtin_amdgcn_exp2f(s[2 * j] - m), __builtin_amdgcn_exp2f(s[2 * j + 1] - m));
#pragma unroll
    for (int j = 0; j < 4; ++j) pb[j] = pk_bf16(__builtin_amdgcn_exp2f(s[8 + 2 * j] - m), __builtin_amdgcn_exp2f(s[8 + 2 * j + 1] - m));
    o = mfma32(va, pa, o);
    o = mfma32(vb, pb, o);
  }
  o_out = o;
}

__global__ __launch_bounds__(512, 2) void fused(const char* __restrict__ x, const char* __restrict__ wqkv, const char* __restrict__ wo,
                                                const char* __restrict__ bk, const char* __restrict__ bv, const char* __restrict__ bsk,
                                                const char* __restrict__ ssm_k, const char* __restrict__ ssm_D, const char* __restrict__ gate_w,
                                                char* ws, char* __restrict__ out) {
  __shared__ __attribute__((aligned(16))) char smem[L_TOTAL];
  const int tid = threadIdx.x, lane = tid & 63, wave = __builtin_amdgcn_readfirstlane(tid >> 6);
  const int b = blockIdx.x, xcd = b & 7, rr = b >> 3;
  const int rb = (xcd >> 1) * 4 + (rr & 3), cb = (xcd & 1) * 8 + (rr >> 2);
  const int head = xcd * 4 + (rr & 3), qblk = rr >> 2, wg = head * 8 + qblk;
  const rsrc_t srd = __builtin_amdgcn_make_buffer_rsrc((void*)ws, 0, WS_RANGE, 0x00020000);
  TS_DECL
  TS(0);
  asm volatile("" :: "s"(x), "s"(wqkv), "s"(bk), "s"(bv), "s"(bsk), "s"(ssm_k), "s"(ssm_D), "s"(ws), "s"(wo), "s"(out), "s"(gate_w));
  u32x2 ev;
  asm volatile("global_load_dwordx2 %0, %1, %2 sc1" : "=&v"(ev) : "v"(WS_E), "s"(ws) : "memory");
  int half = wave >> 2;
  const int kq = wave & 3;
  const bool helper = cb == 15;
  const int chf = cv_channel(wg, (kq & 1) ? 2 : kq >> 1);
  const unsigned dw = *(const __attribute__((address_space(4))) unsigned*)(ssm_D + (chf & ~1) * 2);
  const int r = lane & 31, hh = lane >> 5;
  const int hp = head >> 1, ho = head & 1;
  const int c1 = hp * 5 + (ho ? 3 : 0) + hh, c2 = hp * 5 + 2;
  const int q0 = qblk * 256 + half * 32, tq = q0 + kq * 64 + r;
  const unsigned o_off1 = WS_O + (c1 * 2048 + tq) * 16, o_off2 = hh ? WS_RANGE : WS_O + (c2 * 2048 + tq) * 16 + ho * 8;
  const int t0 = xcd * 256 + (rr & 3) * 64, kc = kq >> 1, tc = (wave & 1) * 16;
  u32x4 xv[2]; u32x2 xw[2];
  unsigned vq1, vq2, vk1, vk2, vv0, vv1, fa, gqu, giu[2];
  f32x16 o[4];
  auto mkfa = [&]() {
    const int kind = lane >> 4, cbx = (head * 20 + ((lane & 8) ? 19 : 0)) >> 6;
    const int frb = kind < 2 ? kq * 8 + (lane & 7) : kind == 3 ? (lane & 15) * 2 : qblk * 4 + (lane & 3);
    fa = WS_FA + ((kind < 2 ? kind * 10 + 10 + cbx : kind == 3 ? 30 : cbx) * 32 + frb) * FST;
  };
  auto prep = [&]() {
    vq1 = WS_QK + (c1 * 2048 + q0 + r) * 16; vq2 = WS_QK + (c2 * 2048 + q0 + r) * 16 + ho * 8;
    vk1 = WS_QK + ((80 + c1) * 2048 + r) * 16; vk2 = WS_QK + ((80 + c2) * 2048 + lane) * 16 + ho * 8;
    int dim;
    if (r < 16) dim = (r & 3) + 4 * ((r >> 3) & 1) + 8 * ((r >> 2) & 1) + (ho ? 4 : 0);
    else dim = ho ? r - 16 : r;
    const unsigned vvb = r > 20 ? (32u << 20) : r == 20 ? WS_V + 32 * 64 * 1280 : WS_V + head * 64 * 1280 + (dim >> 2) * 128 + hh * 64 + (dim & 3) * 16;
    vv0 = vvb - 640; vv1 = vvb + 2560;
    if (!EARLYPOLL || half) mkfa();
    gqu = WS_G + tq * 4;
    const f32x16 z = {};
#pragma unroll
    for (int i = 0; i < 4; ++i) o[i] = z;
    asm volatile("" : "+v"(o[0]), "+v"(o[1]), "+v"(o[2]), "+v"(o[3]), "+v"(vv0), "+v"(vv1), "+v"(vq1), "+v"(vq2), "+v"(vk1), "+v"(vk2), "+v"(fa), "+v"(gqu));
  };
  Stage S;
  u64 tag;
  if (half == 0) {
    kv_issue(bk, bv, bsk, lds_addr(smem) + L_KV, wg, kq, lane);
    TSB(5);
    {
      const char* pc; unsigned len;
      asm volatile("s_getpc_b64 %0\n\ts_mov_b32 %1, fused_code_end-." : "=s"(pc), "=s"(len));
      const unsigned o = (rr * 16 + (lane & 15)) * 128;
      touch(pc, o < len ? o : len, smem);
    }
    if (tid < 16) *(unsigned*)(smem + L_SYNC + tid * 4) = 0;
  }
  asm volatile("" : "+s"(half) :: "memory");
  if (half == 0) {
    WAIT_VM(1);
    TSB(6);
    __builtin_amdgcn_s_barrier();
    tag = make_tag(ev);
    kv_math(lds_addr(smem) + L_KV, srd, wg, kq, lane);
    TSB(7);
    if (__builtin_expect(helper, 0)) {
      WAIT_VM(0);
      RAW_BARRIER();
      TS(2);
      if (tid == 0) post_flag(ws, WS_FU + wg * FUST, tag);
      {
        const u32x4 one = {0x3f803f80u, 0x3f803f80u, 0x3f803f80u, 0x3f803f80u};
        st16(srd, WS_V + (32 * 64 + rb * 4 + wave) * 1280 + (lane >> 2 & 1) * 640 + (lane & 3) * 16, one);
      }
      gate_rows(x, gate_w, srd, rb, wave, lane);
      drain_signal(smem, ws, SY_A, 4, WS_FA + (30 * 32 + rb * 2) * FST, tag, lane);
    } else {
      __builtin_amdgcn_s_setprio(2);
      if (cb < 10) phase_a<false>(smem, ws, srd, tag, rb, cb, wg, wave, lane TS_PASS);
      else phase_a<true>(smem, ws, srd, tag, rb, cb, wg, wave, lane TS_PASS);
      TS(4);
      lds_arrive(smem, SY_A, lane);
      WAIT_VM(0);
      if (lane == 0) post_flag(ws, WS_FA + ((cb * 2 + (wave >> 1)) * 32 + rb * 2 + (wave & 1)) * FST, tag);
      __builtin_amdgcn_s_setprio(OPRIO);
    }
  } else {
    if (!helper) {
      Dma D;
      const int xrow = kq * 8 + (lane >> 3), wrow = kq * 4 + (lane >> 4);
      D.x = x; D.w = wqkv; D.s = ROT_EXPR; D.la = lds_addr(smem) + kq * 1024;
      D.vx = (rb * 128 + xrow) * 1280 + (((lane & 7) ^ ((xrow >> 1) & 7)) << 4);
      D.vw = wrow * 3840 + cb * 256 + (((lane & 15) ^ (((wrow & 3) << 2) | kq)) << 4);
#pragma unroll 1
      for (int k = 0; k < 10; ++k) {
        dma_stage(D, k);
        if (k == 0) TSA(5); if (k == 1) TSA(7); if (k == 2) TSA(10); if (k == 6) TSA(13);
        if (k == (DEEP ? DEEP : 9)) continue;
        if (DEEP && k > DEEP) WAIT_VM(16);
        else WAIT_VM(8);
        if (k == 1) TSA(8); if (k == 2) TSA(11); if (k == 6) TSA(14);
        __builtin_amdgcn_s_barrier();
        if (k == 0) TSA(6); if (k == 1) TSA(9); if (k == 5) TSA(12); if (k == 6) TSA(15);
      }
    }
    TSB(11);
    h_issue(S, ssm_k, wg, tid & 255);
    u_poll_issue(S, ws, wg, lane);
    WAIT_VM(15);
    __builtin_amdgcn_s_barrier();
    TSB(8);
    WAIT_VM(7);
    __builtin_amdgcn_s_barrier();
    TSB(9);
    WAIT_VM(0);
    tag = make_tag(ev);
    asm volatile("" : "+v"(S.pf));
    if (!__all((((u64)S.pf[1] << 32) | S.pf[0]) == tag)) wait_flags(ws, u_flag_off(wg, lane), tag);
    u_issue(S, ws, wg, tid & 255);
    TSB(12);
  }
  if (half) {
    asm volatile("" :: "s"(dw));
    h_write(S, smem, tid & 255);
    prep();
    unsigned vo[5];
#pragma unroll
    for (int j = 0; j < 5; ++j) {
      const int n = j * 64 + lane, rho = n / 10, c = n - rho * 10;
      vo[j] = (rho ^ (((rho >> 3) & 1) << 2)) * 1280 + c * 16;
    }
    asm volatile("" : "+v"(vo[0]), "+v"(vo[1]), "+v"(vo[2]), "+v"(vo[3]), "+v"(vo[4]));
    TSB(10);
    WAIT_VM(0);
    u_write(S, smem, tid & 255);
    lds_arrive(smem, SY_S, lane);
    if (WOTOUCH == 1) touch(wo, min(rr * 200 + (tid & 255), 6399) * 128, smem);
    __builtin_amdgcn_s_setprio(CVPRIO);
    lds_wait(smem, SY_S, 4);
    TS(4);
    const int ci = kq >> 1;
    const char* base = smem + L_CV + ci * CV_CH;
    char* red = smem + ci * 8192;
    const float dv = (chf & 1) ? bf_hi(dw) : bf_lo(dw);
    f32x4 acc[8] = {};
    conv_run<255>(base, (kq & 1) ? 5 : 0, (kq & 1) ? 8 : 5, lane, acc);
    if (WOTOUCH == 2) touch(wo, min(rr * 200 + (tid & 255), 6399) * 128, smem);
    if (kq & 1) {
      conv_send<255>(red, lane, acc);
      lds_arrive(smem, SY_C0 + ci, lane);
      const char* bs = smem + L_CV + 2 * CV_CH;
      char* rs = smem + 16384;
      f32x4 a2[8] = {};
      if (wg & 1) {
        conv_run<0x3B>(bs, ci * 4, ci * 4 + 4, lane, a2);
        if (ci) { conv_send<0x3B>(rs, lane, a2); lds_arrive(smem, SY_CS, lane); }
        else { lds_wait(smem, SY_CS, 1); conv_final<0x3B>(rs, bs, srd, dv, chf, lane, a2); }
      } else {
        conv_run<0xC4>(bs, ci * 4, ci * 4 + 4, lane, a2);
        if (ci) { conv_send<0xC4>(rs, lane, a2); lds_arrive(smem, SY_CS, lane); }
        else { lds_wait(smem, SY_CS, 1); conv_final<0xC4>(rs, bs, srd, dv, chf, lane, a2); }
      }
    } else {
      lds_wait(smem, SY_C0 + ci, 1);
      conv_final<255>(red, base, srd, dv, chf, lane, acc);
    }
    {
      const u32x4 s4 = {~0u, ~0u, ~0u, ~0u};
      const u32x2 s2 = {~0u, ~0u};
      st16(srd, o_off1, s4);
      st8(srd, o_off2, s2);
    }
    TS(5);
    __builtin_amdgcn_s_setprio(YPRIO);
    drain_signal(smem, ws, SY_Z, 4, WS_FZ + wg * FUST, tag, lane);
    lds_wait(smem, SY_A, 4);
    lds_wait(smem, SY_Z, 4);
    const char* src = wo + kq * 5 * 40960 + qblk * 160;
    unsigned la = lds_addr(smem + L_WO) + kq * 5 * 5120;
#pragma unroll 1
    for (int i = 0; i < 5; ++i) {
#pragma unroll
      for (int j = 0; j < 5; ++j) {
        glds(src, vo[j], la + j * 1024);
        __builtin_amdgcn_s_sleep(WOSLEEP);
      }
      src += 40960; la += 5120;
    }
  }
  TS(6);

  u32x2 pf = {0, 0};
  if (half == 0) {
    if (EARLYPOLL) {
      mkfa();
      asm volatile("global_load_dwordx2 %0, %1, %2 sc1" : "=&v"(pf) : "v"(fa), "s"(ws) : "memory");
    }
    giu[0] = WS_G + (t0 + tc + (lane & 15)) * 4;
    const unsigned op = (t0 + tc + (lane & 15)) * 1280 + qblk * 160 + kc * 96 + (lane >> 4) * 16;
    asm volatile("global_load_dwordx4 %0, %4, %6\n\tglobal_load_dwordx2 %1, %5, %6\n\tglobal_load_dwordx4 %2, %4, %7\n\tglobal_load_dwordx2 %3, %5, %7"
                 : "=&v"(xv[0]), "=&v"(xw[0]), "=&v"(xv[1]), "=&v"(xw[1]) : "v"(op), "v"(op + 64 - kc * 96 - (lane >> 4) * 8), "s"(x), "s"(x + 32 * 1280) : "memory");
    asm volatile("" : "+v"(giu[0]));
    prep();
    if (EARLYPOLL) { WAIT_VM(4); asm volatile("" : "+v"(pf)); }
  }
  {
    {
      if (FDELAY && half == 0) __builtin_amdgcn_s_sleep(FDELAY);
      if (!EARLYPOLL || half || !__all((((u64)pf[1] << 32) | pf[0]) == tag)) wait_flags(ws, fa, tag);
    }
    TS(7);
    if (half == 0) {
      __builtin_amdgcn_s_setprio(2);
      giu[1] = ld4(srd, giu[0] + 128);
      giu[0] = ld4(srd, giu[0]);
    } else {
      WAIT_VM(0);
      lds_arrive(smem, SY_W, lane);
    }
    gqu = ld4(srd, gqu);
    unsigned sk = __builtin_amdgcn_readfirstlane(kq * 8 * 1024), sv = __builtin_amdgcn_readfirstlane(kq * 8 * 2560), cnt = 4;
    const unsigned oob = WS_RANGE;
    u32x4 q1[4]; u32x2 qa[4];
    asm volatile(
      "s_nop 4\n"
      "buffer_load_dwordx2 %[qa0], %[vq2], %[srd], 0 offen offset:0 sc1\n"
      "buffer_load_dwordx4 %[q10], %[vq1], %[srd], 0 offen offset:0 sc1\n"
      "buffer_load_dwordx2 %[qa1], %[vq2], %[srd], 0 offen offset:1024 sc1\n"
      "buffer_load_dwordx4 %[q11], %[vq1], %[srd], 0 offen offset:1024 sc1\n"
      "buffer_load_dwordx2 %[qa2], %[vq2], %[srd], 0 offen offset:2048 sc1\n"
      "buffer_load_dwordx4 %[q12], %[vq1], %[srd], 0 offen offset:2048 sc1\n"
      "buffer_load_dwordx2 %[qa3], %[vq2], %[srd], 0 offen offset:3072 sc1\n"
      "buffer_load_dwordx4 %[q13], %[vq1], %[srd], 0 offen offset:3072 sc1\n"
      "buffer_load_dwordx2 v[228:229], %[vk2], %[srd], %[sk] offen offset:0 sc1\n"
      "buffer_load_dwordx4 v[204:207], %[vk1], %[srd], %[sk] offen offset:0 sc1\n"
      "buffer_load_dwordx4 v[208:211], %[vk1], %[srd], %[sk] offen offset:512 sc1\n"
      "buffer_load_dwordx4 v[212:215], %[vv0], %[srd], %[sv] offen offset:640 sc1\n"
      "buffer_load_dwordx4 v[216:219], %[vv0], %[srd], %[sv] offen offset:1280 sc1\n"
      "buffer_load_dwordx4 v[220:223], %[vv0], %[srd], %[sv] offen offset:1920 sc1\n"
      "buffer_load_dwordx4 v[224:227], %[vv0], %[srd], %[sv] offen offset:2560 sc1\n"
      "buffer_load_dwordx2 v[254:255], %[vk2], %[srd], %[sk] offen offset:1024 sc1\n"
      "buffer_load_dwordx4 v[238:241], %[vv1], %[srd], %[sv] offen offset:0 sc1\n"
      "buffer_load_dwordx4 v[230:233], %[vk1], %[srd], %[sk] offen offset:1024 sc1\n"
      "buffer_load_dwordx4 v[242:245], %[vv1], %[srd], %[sv] offen offset:640 sc1\n"
      "buffer_load_dwordx4 v[234:237], %[vk1], %[srd], %[sk] offen offset:1536 sc1\n"
      "buffer_load_dwordx4 v[246:249], %[vv1], %[srd], %[sv] offen offset:1280 sc1\n"
      "s_add_u32 %[sk], %[sk], 2048\n"
      "s_add_u32 %[sv], %[sv], 5120\n"
      "v_mov_b32 v200, 0\n"
      "v_mov_b32 v250, 0\n"
      "v_mov_b32 v201, 0\n"
      "v_mov_b32 v251, 0\n"
      "v_mov_b32 v202, 0\n"
      "v_mov_b32 v252, 0\n"
      "v_mov_b32 v203, 0\n"
      "v_mov_b32 v253, 0\n"
      "s_waitcnt vmcnt(12)\n"
      "v_mfma_f32_32x32x4_2b_bf16 v[132:163], v[228:229], %[qa0], 0\n"
      "s_nop 15\n"
      "s_nop 1\n"
      "s_waitcnt vmcnt(10)\n"
      "v_mfma_f32_32x32x16_bf16 v[132:147], v[204:207], %[q10], v[132:147]\n"
      "v_mfma_f32_32x32x16_bf16 v[148:163], v[208:211], %[q10], v[148:163]\n"
      "s_nop 11\n"
      "1:\n"
      "v_mfma_f32_32x32x4_2b_bf16 v[164:195], v[228:229], %[qa1], 0\n"
      "v_exp_f32 v132, v132\n"
      "v_exp_f32 v133, v133\n"
      "v_exp_f32 v134, v134\n"
      "v_exp_f32 v135, v135\n"
      "v_exp_f32 v136, v136\n"
      "v_exp_f32 v137, v137\n"
      "v_exp_f32 v138, v138\n"
      "v_exp_f32 v139, v139\n"
      "v_mfma_f32_32x32x16_bf16 %[o3], v[250:253], v[200:203], %[o3]\n"
      "buffer_load_dwordx4 v[250:253], %[vv0], %[srd], %[sv] offen offset:0 sc1\n"
      "v_cvt_pk_bf16_f32 v196, v132, v133\n"
      "v_cvt_pk_bf16_f32 v197, v134, v135\n"
      "v_cvt_pk_bf16_f32 v198, v136, v137\n"
      "v_cvt_pk_bf16_f32 v199, v138, v139\n"
      "s_cmp_eq_u32 %[cnt], 1\n"
      "s_cselect_b32 %[sk], %[oob], %[sk]\n"
      "s_cselect_b32 %[sv], %[oob], %[sv]\n"
      "v_exp_f32 v140, v140\n"
      "v_exp_f32 v141, v141\n"
      "v_exp_f32 v142, v142\n"
      "s_waitcnt vmcnt(10)\n"
      "v_mfma_f32_32x32x16_bf16 %[o0], v[212:215], v[196:199], %[o0]\n"
      "v_exp_f32 v143, v143\n"
      "v_exp_f32 v144, v144\n"
      "v_exp_f32 v145, v145\n"
      "v_exp_f32 v146, v146\n"
      "v_exp_f32 v147, v147\n"
      "v_mfma_f32_32x32x16_bf16 v[164:179], v[204:207], %[q11], v[164:179]\n"
      "v_cvt_pk_bf16_f32 v200, v140, v141\n"
      "v_cvt_pk_bf16_f32 v201, v142, v143\n"
      "v_cvt_pk_bf16_f32 v202, v144, v145\n"
      "v_cvt_pk_bf16_f32 v203, v146, v147\n"
      "v_exp_f32 v148, v148\n"
      "v_exp_f32 v149, v149\n"
      "v_exp_f32 v150, v150\n"
      "s_waitcnt vmcnt(9)\n"
      "v_mfma_f32_32x32x16_bf16 %[o0], v[216:219], v[200:203], %[o0]\n"
      "v_exp_f32 v151, v151\n"
      "v_exp_f32 v152, v152\n"
      "v_exp_f32 v153, v153\n"
      "v_exp_f32 v154, v154\n"
      "v_exp_f32 v155, v155\n"
      "v_mfma_f32_32x32x16_bf16 v[180:195], v[208:211], %[q11], v[180:195]\n"
      "v_cvt_pk_bf16_f32 v196, v148, v149\n"
      "v_cvt_pk_bf16_f32 v197, v150, v151\n"
      "v_cvt_pk_bf16_f32 v198, v152, v153\n"
      "v_cvt_pk_bf16_f32 v199, v154, v155\n"
      "v_exp_f32 v156, v156\n"
      "v_exp_f32 v157, v157\n"
      "v_exp_f32 v158, v158\n"
      "v_exp_f32 v159, v159\n"
      "s_waitcnt vmcnt(8)\n"
      "v_mfma_f32_32x32x16_bf16 %[o0], v[220:223], v[196:199], %[o0]\n"
      "v_exp_f32 v160, v160\n"
      "v_exp_f32 v161, v161\n"
      "v_exp_f32 v162, v162\n"
      "v_exp_f32 v163, v163\n"
      "v_cvt_pk_bf16_f32 v200, v156, v157\n"
      "v_cvt_pk_bf16_f32 v201, v158, v159\n"
      "v_cvt_pk_bf16_f32 v202, v160, v161\n"
      "v_cvt_pk_bf16_f32 v203, v162, v163\n"
      "v_mfma_f32_32x32x4_2b_bf16 v[132:163], v[228:229], %[qa2], 0\n"
      "v_exp_f32 v164, v164\n"
      "v_exp_f32 v165, v165\n"
      "v_exp_f32 v166, v166\n"
      "v_exp_f32 v167, v167\n"
      "v_exp_f32 v168, v168\n"
      "v_exp_f32 v169, v169\n"
      "v_exp_f32 v170, v170\n"
      "v_exp_f32 v171, v171\n"
      "s_waitcnt vmcnt(7)\n"
      "v_mfma_f32_32x32x16_bf16 %[o0], v[224:227], v[200:203], %[o0]\n"
      "v_cvt_pk_bf16_f32 v196, v164, v165\n"
      "v_cvt_pk_bf16_f32 v197, v166, v167\n"
      "v_cvt_pk_bf16_f32 v198, v168, v169\n"
      "v_cvt_pk_bf16_f32 v199, v170, v171\n"
      "v_exp_f32 v172, v172\n"
      "v_exp_f32 v173, v173\n"
      "v_exp_f32 v174, v174\n"
      "v_mfma_f32_32x32x16_bf16 %[o1], v[212:215], v[196:199], %[o1]\n"
      "v_exp_f32 v175, v175\n"
      "v_exp_f32 v176, v176\n"
      "v_exp_f32 v177, v177\n"
      "v_exp_f32 v178, v178\n"
      "v_exp_f32 v179, v179\n"
      "v_mfma_f32_32x32x16_bf16 v[132:147], v[204:207], %[q12], v[132:147]\n"
      "v_cvt_pk_bf16_f32 v200, v172, v173\n"
      "v_cvt_pk_bf16_f32 v201, v174, v175\n"
      "v_cvt_pk_bf16_f32 v202, v176, v177\n"
      "v_cvt_pk_bf16_f32 v203, v178, v179\n"
      "v_exp_f32 v180, v180\n"
      "v_exp_f32 v181, v181\n"
      "v_exp_f32 v182, v182\n"
      "v_mfma_f32_32x32x16_bf16 %[o1], v[216:219], v[200:203], %[o1]\n"
      "v_exp_f32 v183, v183\n"
      "v_exp_f32 v184, v184\n"
      "v_exp_f32 v185, v185\n"
      "v_exp_f32 v186, v186\n"
      "v_exp_f32 v187, v187\n"
      "v_mfma_f32_32x32x16_bf16 v[148:163], v[208:211], %[q12], v[148:163]\n"
      "v_cvt_pk_bf16_f32 v196, v180, v181\n"
      "v_cvt_pk_bf16_f32 v197, v182, v183\n"
      "v_cvt_pk_bf16_f32 v198, v184, v185\n"
      "v_cvt_pk_bf16_f32 v199, v186, v187\n"
      "v_exp_f32 v188, v188\n"
      "v_exp_f32 v189, v189\n"
      "v_exp_f32 v190, v190\n"
      "v_exp_f32 v191, v191\n"
      "v_mfma_f32_32x32x16_bf16 %[o1], v[220:223], v[196:199], %[o1]\n"
      "v_exp_f32 v192, v192\n"
      "v_exp_f32 v193, v193\n"
      "v_exp_f32 v194, v194\n"
      "v_exp_f32 v195, v195\n"
      "v_cvt_pk_bf16_f32 v200, v188, v189\n"
      "v_cvt_pk_bf16_f32 v201, v190, v191\n"
      "v_cvt_pk_bf16_f32 v202, v192, v193\n"
      "v_cvt_pk_bf16_f32 v203, v194, v195\n"
      "v_mfma_f32_32x32x4_2b_bf16 v[164:195], v[228:229], %[qa3], 0\n"
      "buffer_load_dwordx2 v[228:229], %[vk2], %[srd], %[sk] offen offset:0 sc1\n"
      "v_exp_f32 v132, v132\n"
      "v_exp_f32 v133, v133\n"
      "v_exp_f32 v134, v134\n"
      "v_exp_f32 v135, v135\n"
      "v_exp_f32 v136, v136\n"
      "v_exp_f32 v137, v137\n"
      "v_exp_f32 v138, v138\n"
      "v_exp_f32 v139, v139\n"
      "v_mfma_f32_32x32x16_bf16 %[o1], v[224:227], v[200:203], %[o1]\n"
      "v_cvt_pk_bf16_f32 v196, v132, v133\n"
      "v_cvt_pk_bf16_f32 v197, v134, v135\n"
      "v_cvt_pk_bf16_f32 v198, v136, v137\n"
      "v_cvt_pk_bf16_f32 v199, v138, v139\n"
      "v_exp_f32 v140, v140\n"
      "v_exp_f32 v141, v141\n"
      "v_exp_f32 v142, v142\n"
      "v_mfma_f32_32x32x16_bf16 %[o2], v[212:215], v[196:199], %[o2]\n"
      "v_exp_f32 v143, v143\n"
      "v_exp_f32 v144, v144\n"
      "v_exp_f32 v145, v145\n"
      "v_exp_f32 v146, v146\n"
      "v_exp_f32 v147, v147\n"
      "v_mfma_f32_32x32x16_bf16 v[164:179], v[204:207], %[q13], v[164:179]\n"
      "buffer_load_dwordx4 v[204:207], %[vk1], %[srd], %[sk] offen offset:0 sc1\n"
      "v_cvt_pk_bf16_f32 v200, v140, v141\n"
      "v_cvt_pk_bf16_f32 v201, v142, v143\n"
      "v_cvt_pk_bf16_f32 v202, v144, v145\n"
      "v_cvt_pk_bf16_f32 v203, v146, v147\n"
      "v_exp_f32 v148, v148\n"
      "v_exp_f32 v149, v149\n"
      "v_exp_f32 v150, v150\n"
      "v_mfma_f32_32x32x16_bf16 %[o2], v[216:219], v[200:203], %[o2]\n"
      "v_exp_f32 v151, v151\n"
      "v_exp_f32 v152, v152\n"
      "v_exp_f32 v153, v153\n"
      "v_exp_f32 v154, v154\n"
      "v_exp_f32 v155, v155\n"
      "v_mfma_f32_32x32x16_bf16 v[180:195], v[208:211], %[q13], v[180:195]\n"
      "buffer_load_dwordx4 v[208:211], %[vk1], %[srd], %[sk] offen offset:512 sc1\n"
      "v_cvt_pk_bf16_f32 v196, v148, v149\n"
      "v_cvt_pk_bf16_f32 v197, v150, v151\n"
      "v_cvt_pk_bf16_f32 v198, v152, v153\n"
      "v_cvt_pk_bf16_f32 v199, v154, v155\n"
      "v_exp_f32 v156, v156\n"
      "v_exp_f32 v157, v157\n"
      "v_exp_f32 v158, v158\n"
      "v_exp_f32 v159, v159\n"
      "v_mfma_f32_32x32x16_bf16 %[o2], v[220:223], v[196:199], %[o2]\n"
      "v_exp_f32 v160, v160\n"
      "v_exp_f32 v161, v161\n"
      "v_exp_f32 v162, v162\n"
      "v_exp_f32 v163, v163\n"
      "v_cvt_pk_bf16_f32 v200, v156, v157\n"
      "v_cvt_pk_bf16_f32 v201, v158, v159\n"
      "v_cvt_pk_bf16_f32 v202, v160, v161\n"
      "v_cvt_pk_bf16_f32 v203, v162, v163\n"
      "s_waitcnt vmcnt(9)\n"
      "v_mfma_f32_32x32x4_2b_bf16 v[132:163], v[254:255], %[qa0], 0\n"
      "v_exp_f32 v164, v164\n"
      "v_exp_f32 v165, v165\n"
      "v_exp_f32 v166, v166\n"
      "v_exp_f32 v167, v167\n"
      "v_exp_f32 v168, v168\n"
      "v_exp_f32 v169, v169\n"
      "v_exp_f32 v170, v170\n"
      "v_exp_f32 v171, v171\n"
      "v_mfma_f32_32x32x16_bf16 %[o2], v[224:227], v[200:203], %[o2]\n"
      "v_cvt_pk_bf16_f32 v196, v164, v165\n"
      "v_cvt_pk_bf16_f32 v197, v166, v167\n"
      "v_cvt_pk_bf16_f32 v198, v168, v169\n"
      "v_cvt_pk_bf16_f32 v199, v170, v171\n"
      "v_exp_f32 v172, v172\n"
      "v_exp_f32 v173, v173\n"
      "v_exp_f32 v174, v174\n"
      "v_mfma_f32_32x32x16_bf16 %[o3], v[212:215], v[196:199], %[o3]\n"
      "buffer_load_dwordx4 v[212:215], %[vv0], %[srd], %[sv] offen offset:640 sc1\n"
      "v_exp_f32 v175, v175\n"
      "v_exp_f32 v176, v176\n"
      "v_exp_f32 v177, v177\n"
      "v_exp_f32 v178, v178\n"
      "v_exp_f32 v179, v179\n"
      "s_waitcnt vmcnt(8)\n"
      "v_mfma_f32_32x32x16_bf16 v[132:147], v[230:233], %[q10], v[132:147]\n"
      "v_cvt_pk_bf16_f32 v200, v172, v173\n"
      "v_cvt_pk_bf16_f32 v201, v174, v175\n"
      "v_cvt_pk_bf16_f32 v202, v176, v177\n"
      "v_cvt_pk_bf16_f32 v203, v178, v179\n"
      "v_exp_f32 v180, v180\n"
      "v_exp_f32 v181, v181\n"
      "v_exp_f32 v182, v182\n"
      "v_mfma_f32_32x32x16_bf16 %[o3], v[216:219], v[200:203], %[o3]\n"
      "buffer_load_dwordx4 v[216:219], %[vv0], %[srd], %[sv] offen offset:1280 sc1\n"
      "v_exp_f32 v183, v183\n"
      "v_exp_f32 v184, v184\n"
      "v_exp_f32 v185, v185\n"
      "v_exp_f32 v186, v186\n"
      "v_exp_f32 v187, v187\n"
      "s_waitcnt vmcnt(7)\n"
      "v_mfma_f32_32x32x16_bf16 v[148:163], v[234:237], %[q10], v[148:163]\n"
      "v_cvt_pk_bf16_f32 v196, v180, v181\n"
      "v_cvt_pk_bf16_f32 v197, v182, v183\n"
      "v_cvt_pk_bf16_f32 v198, v184, v185\n"
      "v_cvt_pk_bf16_f32 v199, v186, v187\n"
      "v_exp_f32 v188, v188\n"
      "v_exp_f32 v189, v189\n"
      "v_exp_f32 v190, v190\n"
      "v_exp_f32 v191, v191\n"
      "v_mfma_f32_32x32x16_bf16 %[o3], v[220:223], v[196:199], %[o3]\n"
      "buffer_load_dwordx4 v[220:223], %[vv0], %[srd], %[sv] offen offset:1920 sc1\n"
      "v_exp_f32 v192, v192\n"
      "v_exp_f32 v193, v193\n"
      "v_exp_f32 v194, v194\n"
      "v_exp_f32 v195, v195\n"
      "v_cvt_pk_bf16_f32 v200, v188, v189\n"
      "v_cvt_pk_bf16_f32 v201, v190, v191\n"
      "v_cvt_pk_bf16_f32 v202, v192, v193\n"
      "v_cvt_pk_bf16_f32 v203, v194, v195\n"
      "v_mfma_f32_32x32x4_2b_bf16 v[164:195], v[254:255], %[qa1], 0\n"
      "v_exp_f32 v132, v132\n"
      "v_exp_f32 v133, v133\n"
      "v_exp_f32 v134, v134\n"
      "v_exp_f32 v135, v135\n"
      "v_exp_f32 v136, v136\n"
      "v_exp_f32 v137, v137\n"
      "v_exp_f32 v138, v138\n"
      "v_exp_f32 v139, v139\n"
      "v_mfma_f32_32x32x16_bf16 %[o3], v[224:227], v[200:203], %[o3]\n"
      "buffer_load_dwordx4 v[224:227], %[vv0], %[srd], %[sv] offen offset:2560 sc1\n"
      "v_cvt_pk_bf16_f32 v196, v132, v133\n"
      "v_cvt_pk_bf16_f32 v197, v134, v135\n"
      "v_cvt_pk_bf16_f32 v198, v136, v137\n"
      "v_cvt_pk_bf16_f32 v199, v138, v139\n"
      "v_exp_f32 v140, v140\n"
      "v_exp_f32 v141, v141\n"
      "v_exp_f32 v142, v142\n"
      "s_waitcnt vmcnt(10)\n"
      "v_mfma_f32_32x32x16_bf16 %[o0], v[238:241], v[196:199], %[o0]\n"
      "v_exp_f32 v143, v143\n"
      "v_exp_f32 v144, v144\n"
      "v_exp_f32 v145, v145\n"
      "v_exp_f32 v146, v146\n"
      "v_exp_f32 v147, v147\n"
      "v_mfma_f32_32x32x16_bf16 v[164:179], v[230:233], %[q11], v[164:179]\n"
      "v_cvt_pk_bf16_f32 v200, v140, v141\n"
      "v_cvt_pk_bf16_f32 v201, v142, v143\n"
      "v_cvt_pk_bf16_f32 v202, v144, v145\n"
      "v_cvt_pk_bf16_f32 v203, v146, v147\n"
      "v_exp_f32 v148, v148\n"
      "v_exp_f32 v149, v149\n"
      "v_exp_f32 v150, v150\n"
      "s_waitcnt vmcnt(9)\n"
      "v_mfma_f32_32x32x16_bf16 %[o0], v[242:245], v[200:203], %[o0]\n"
      "v_exp_f32 v151, v151\n"
      "v_exp_f32 v152, v152\n"
      "v_exp_f32 v153, v153\n"
      "v_exp_f32 v154, v154\n"
      "v_exp_f32 v155, v155\n"
      "v_mfma_f32_32x32x16_bf16 v[180:195], v[234:237], %[q11], v[180:195]\n"
      "v_cvt_pk_bf16_f32 v196, v148, v149\n"
      "v_cvt_pk_bf16_f32 v197, v150, v151\n"
      "v_cvt_pk_bf16_f32 v198, v152, v153\n"
      "v_cvt_pk_bf16_f32 v199, v154, v155\n"
      "v_exp_f32 v156, v156\n"
      "v_exp_f32 v157, v157\n"
      "v_exp_f32 v158, v158\n"
      "v_exp_f32 v159, v159\n"
      "s_waitcnt vmcnt(8)\n"
      "v_mfma_f32_32x32x16_bf16 %[o0], v[246:249], v[196:199], %[o0]\n"
      "v_exp_f32 v160, v160\n"
      "v_exp_f32 v161, v161\n"
      "v_exp_f32 v162, v162\n"
      "v_exp_f32 v163, v163\n"
      "v_cvt_pk_bf16_f32 v200, v156, v157\n"
      "v_cvt_pk_bf16_f32 v201, v158, v159\n"
      "v_cvt_pk_bf16_f32 v202, v160, v161\n"
      "v_cvt_pk_bf16_f32 v203, v162, v163\n"
      "v_mfma_f32_32x32x4_2b_bf16 v[132:163], v[254:255], %[qa2], 0\n"
      "v_exp_f32 v164, v164\n"
      "v_exp_f32 v165, v165\n"
      "v_exp_f32 v166, v166\n"
      "v_exp_f32 v167, v167\n"
      "v_exp_f32 v168, v168\n"
      "v_exp_f32 v169, v169\n"
      "v_exp_f32 v170, v170\n"
      "v_exp_f32 v171, v171\n"
      "s_waitcnt vmcnt(7)\n"
      "v_mfma_f32_32x32x16_bf16 %[o0], v[250:253], v[200:203], %[o0]\n"
      "v_cvt_pk_bf16_f32 v196, v164, v165\n"
      "v_cvt_pk_bf16_f32 v197, v166, v167\n"
      "v_cvt_pk_bf16_f32 v198, v168, v169\n"
      "v_cvt_pk_bf16_f32 v199, v170, v171\n"
      "v_exp_f32 v172, v172\n"
      "v_exp_f32 v173, v173\n"
      "v_exp_f32 v174, v174\n"
      "v_mfma_f32_32x32x16_bf16 %[o1], v[238:241], v[196:199], %[o1]\n"
      "v_exp_f32 v175, v175\n"
      "v_exp_f32 v176, v176\n"
      "v_exp_f32 v177, v177\n"
      "v_exp_f32 v178, v178\n"
      "v_exp_f32 v179, v179\n"
      "v_mfma_f32_32x32x16_bf16 v[132:147], v[230:233], %[q12], v[132:147]\n"
      "v_cvt_pk_bf16_f32 v200, v172, v173\n"
      "v_cvt_pk_bf16_f32 v201, v174, v175\n"
      "v_cvt_pk_bf16_f32 v202, v176, v177\n"
      "v_cvt_pk_bf16_f32 v203, v178, v179\n"
      "v_exp_f32 v180, v180\n"
      "v_exp_f32 v181, v181\n"
      "v_exp_f32 v182, v182\n"
      "v_mfma_f32_32x32x16_bf16 %[o1], v[242:245], v[200:203], %[o1]\n"
      "v_exp_f32 v183, v183\n"
      "v_exp_f32 v184, v184\n"
      "v_exp_f32 v185, v185\n"
      "v_exp_f32 v186, v186\n"
      "v_exp_f32 v187, v187\n"
      "v_mfma_f32_32x32x16_bf16 v[148:163], v[234:237], %[q12], v[148:163]\n"
      "v_cvt_pk_bf16_f32 v196, v180, v181\n"
      "v_cvt_pk_bf16_f32 v197, v182, v183\n"
      "v_cvt_pk_bf16_f32 v198, v184, v185\n"
      "v_cvt_pk_bf16_f32 v199, v186, v187\n"
      "v_exp_f32 v188, v188\n"
      "v_exp_f32 v189, v189\n"
      "v_exp_f32 v190, v190\n"
      "v_exp_f32 v191, v191\n"
      "v_mfma_f32_32x32x16_bf16 %[o1], v[246:249], v[196:199], %[o1]\n"
      "v_exp_f32 v192, v192\n"
      "v_exp_f32 v193, v193\n"
      "v_exp_f32 v194, v194\n"
      "v_exp_f32 v195, v195\n"
      "v_cvt_pk_bf16_f32 v200, v188, v189\n"
      "v_cvt_pk_bf16_f32 v201, v190, v191\n"
      "v_cvt_pk_bf16_f32 v202, v192, v193\n"
      "v_cvt_pk_bf16_f32 v203, v194, v195\n"
      "v_mfma_f32_32x32x4_2b_bf16 v[164:195], v[254:255], %[qa3], 0\n"
      "buffer_load_dwordx2 v[254:255], %[vk2], %[srd], %[sk] offen offset:1024 sc1\n"
      "v_exp_f32 v132, v132\n"
      "v_exp_f32 v133, v133\n"
      "v_exp_f32 v134, v134\n"
      "v_exp_f32 v135, v135\n"
      "v_exp_f32 v136, v136\n"
      "v_exp_f32 v137, v137\n"
      "v_exp_f32 v138, v138\n"
      "v_exp_f32 v139, v139\n"
      "v_mfma_f32_32x32x16_bf16 %[o1], v[250:253], v[200:203], %[o1]\n"
      "v_cvt_pk_bf16_f32 v196, v132, v133\n"
      "v_cvt_pk_bf16_f32 v197, v134, v135\n"
      "v_cvt_pk_bf16_f32 v198, v136, v137\n"
      "v_cvt_pk_bf16_f32 v199, v138, v139\n"
      "v_exp_f32 v140, v140\n"
      "v_exp_f32 v141, v141\n"
      "v_exp_f32 v142, v142\n"
      "v_mfma_f32_32x32x16_bf16 %[o2], v[238:241], v[196:199], %[o2]\n"
      "v_exp_f32 v143, v143\n"
      "v_exp_f32 v144, v144\n"
      "v_exp_f32 v145, v145\n"
      "v_exp_f32 v146, v146\n"
      "v_exp_f32 v147, v147\n"
      "v_mfma_f32_32x32x16_bf16 v[164:179], v[230:233], %[q13], v[164:179]\n"
      "buffer_load_dwordx4 v[230:233], %[vk1], %[srd], %[sk] offen offset:1024 sc1\n"
      "v_cvt_pk_bf16_f32 v200, v140, v141\n"
      "v_cvt_pk_bf16_f32 v201, v142, v143\n"
      "v_cvt_pk_bf16_f32 v202, v144, v145\n"
      "v_cvt_pk_bf16_f32 v203, v146, v147\n"
      "v_exp_f32 v148, v148\n"
      "v_exp_f32 v149, v149\n"
      "v_exp_f32 v150, v150\n"
      "v_mfma_f32_32x32x16_bf16 %[o2], v[242:245], v[200:203], %[o2]\n"
      "v_exp_f32 v151, v151\n"
      "v_exp_f32 v152, v152\n"
      "v_exp_f32 v153, v153\n"
      "v_exp_f32 v154, v154\n"
      "v_exp_f32 v155, v155\n"
      "v_mfma_f32_32x32x16_bf16 v[180:195], v[234:237], %[q13], v[180:195]\n"
      "buffer_load_dwordx4 v[234:237], %[vk1], %[srd], %[sk] offen offset:1536 sc1\n"
      "v_cvt_pk_bf16_f32 v196, v148, v149\n"
      "v_cvt_pk_bf16_f32 v197, v150, v151\n"
      "v_cvt_pk_bf16_f32 v198, v152, v153\n"
      "v_cvt_pk_bf16_f32 v199, v154, v155\n"
      "v_exp_f32 v156, v156\n"
      "v_exp_f32 v157, v157\n"
      "v_exp_f32 v158, v158\n"
      "v_exp_f32 v159, v159\n"
      "v_mfma_f32_32x32x16_bf16 %[o2], v[246:249], v[196:199], %[o2]\n"
      "v_exp_f32 v160, v160\n"
      "v_exp_f32 v161, v161\n"
      "v_exp_f32 v162, v162\n"
      "v_exp_f32 v163, v163\n"
      "v_cvt_pk_bf16_f32 v200, v156, v157\n"
      "v_cvt_pk_bf16_f32 v201, v158, v159\n"
      "v_cvt_pk_bf16_f32 v202, v160, v161\n"
      "v_cvt_pk_bf16_f32 v203, v162, v163\n"
      "s_waitcnt vmcnt(9)\n"
      "v_mfma_f32_32x32x4_2b_bf16 v[132:163], v[228:229], %[qa0], 0\n"
      "v_exp_f32 v164, v164\n"
      "v_exp_f32 v165, v165\n"
      "v_exp_f32 v166, v166\n"
      "v_exp_f32 v167, v167\n"
      "v_exp_f32 v168, v168\n"
      "v_exp_f32 v169, v169\n"
      "v_exp_f32 v170, v170\n"
      "v_exp_f32 v171, v171\n"
      "v_mfma_f32_32x32x16_bf16 %[o2], v[250:253], v[200:203], %[o2]\n"
      "v_cvt_pk_bf16_f32 v196, v164, v165\n"
      "v_cvt_pk_bf16_f32 v197, v166, v167\n"
      "v_cvt_pk_bf16_f32 v198, v168, v169\n"
      "v_cvt_pk_bf16_f32 v199, v170, v171\n"
      "v_exp_f32 v172, v172\n"
      "v_exp_f32 v173, v173\n"
      "v_exp_f32 v174, v174\n"
      "v_mfma_f32_32x32x16_bf16 %[o3], v[238:241], v[196:199], %[o3]\n"
      "buffer_load_dwordx4 v[238:241], %[vv1], %[srd], %[sv] offen offset:0 sc1\n"
      "v_exp_f32 v175, v175\n"
      "v_exp_f32 v176, v176\n"
      "v_exp_f32 v177, v177\n"
      "v_exp_f32 v178, v178\n"
      "v_exp_f32 v179, v179\n"
      "s_waitcnt vmcnt(9)\n"
      "v_mfma_f32_32x32x16_bf16 v[132:147], v[204:207], %[q10], v[132:147]\n"
      "v_cvt_pk_bf16_f32 v200, v172, v173\n"
      "v_cvt_pk_bf16_f32 v201, v174, v175\n"
      "v_cvt_pk_bf16_f32 v202, v176, v177\n"
      "v_cvt_pk_bf16_f32 v203, v178, v179\n"
      "v_exp_f32 v180, v180\n"
      "v_exp_f32 v181, v181\n"
      "v_exp_f32 v182, v182\n"
      "v_mfma_f32_32x32x16_bf16 %[o3], v[242:245], v[200:203], %[o3]\n"
      "buffer_load_dwordx4 v[242:245], %[vv1], %[srd], %[sv] offen offset:640 sc1\n"
      "v_exp_f32 v183, v183\n"
      "v_exp_f32 v184, v184\n"
      "v_exp_f32 v185, v185\n"
      "v_exp_f32 v186, v186\n"
      "v_exp_f32 v187, v187\n"
      "s_waitcnt vmcnt(9)\n"
      "v_mfma_f32_32x32x16_bf16 v[148:163], v[208:211], %[q10], v[148:163]\n"
      "v_cvt_pk_bf16_f32 v196, v180, v181\n"
      "v_cvt_pk_bf16_f32 v197, v182, v183\n"
      "v_cvt_pk_bf16_f32 v198, v184, v185\n"
      "v_cvt_pk_bf16_f32 v199, v186, v187\n"
      "v_exp_f32 v188, v188\n"
      "v_exp_f32 v189, v189\n"
      "v_exp_f32 v190, v190\n"
      "v_exp_f32 v191, v191\n"
      "v_mfma_f32_32x32x16_bf16 %[o3], v[246:249], v[196:199], %[o3]\n"
      "buffer_load_dwordx4 v[246:249], %[vv1], %[srd], %[sv] offen offset:1280 sc1\n"
      "v_exp_f32 v192, v192\n"
      "v_exp_f32 v193, v193\n"
      "v_exp_f32 v194, v194\n"
      "v_exp_f32 v195, v195\n"
      "s_add_u32 %[sk], %[sk], 2048\n"
      "s_add_u32 %[sv], %[sv], 5120\n"
      "s_sub_u32 %[cnt], %[cnt], 1\n"
      "s_cmp_lg_u32 %[cnt], 0\n"
      "v_cvt_pk_bf16_f32 v200, v188, v189\n"
      "v_cvt_pk_bf16_f32 v201, v190, v191\n"
      "v_cvt_pk_bf16_f32 v202, v192, v193\n"
      "v_cvt_pk_bf16_f32 v203, v194, v195\n"
      "s_cbranch_scc1 1b\n"
      "s_nop 0\n"
      "v_mfma_f32_32x32x16_bf16 %[o3], v[250:253], v[200:203], %[o3]\n"
      "s_waitcnt vmcnt(0)\n"
      "s_nop 11\n"
      : [o0] "+v"(o[0]), [o1] "+v"(o[1]), [o2] "+v"(o[2]), [o3] "+v"(o[3]), [sk] "+s"(sk), [sv] "+s"(sv), [cnt] "+s"(cnt),
        [q10] "=&v"(q1[0]), [q11] "=&v"(q1[1]), [q12] "=&v"(q1[2]), [q13] "=&v"(q1[3]),
        [qa0] "=&v"(qa[0]), [qa1] "=&v"(qa[1]), [qa2] "=&v"(qa[2]), [qa3] "=&v"(qa[3])
      : [srd] "s"(srd), [oob] "s"(oob), [vk1] "v"(vk1), [vk2] "v"(vk2), [vv0] "v"(vv0), [vv1] "v"(vv1), [vq1] "v"(vq1), [vq2] "v"(vq2)
      : "memory", "scc", "v132", "v133", "v134", "v135", "v136", "v137", "v138", "v139", "v140", "v141", "v142", "v143", "v144", "v145", "v146", "v147", "v148", "v149", "v150", "v151", "v152", "v153", "v154", "v155", "v156", "v157", "v158", "v159", "v160", "v161", "v162", "v163", "v164", "v165", "v166", "v167", "v168", "v169", "v170", "v171", "v172", "v173", "v174", "v175", "v176", "v177", "v178", "v179", "v180", "v181", "v182", "v183", "v184", "v185", "v186", "v187", "v188", "v189", "v190", "v191", "v192", "v193", "v194", "v195", "v196", "v197", "v198", "v199", "v200", "v201", "v202", "v203", "v204", "v205", "v206", "v207", "v208", "v209", "v210", "v211", "v212", "v213", "v214", "v215", "v216", "v217", "v218", "v219", "v220", "v221", "v222", "v223", "v224", "v225", "v226", "v227", "v228", "v229", "v230", "v231", "v232", "v233", "v234", "v235", "v236", "v237", "v238", "v239", "v240", "v241", "v242", "v243", "v244", "v245", "v246", "v247", "v248", "v249", "v250", "v251", "v252", "v253", "v254", "v255"
    );
  }
  asm volatile("" : "+v"(gqu), "+v"(giu[0]), "+v"(giu[1]), "+v"(xv[0]), "+v"(xw[0]), "+v"(xv[1]), "+v"(xw[1]));
  if (half) __builtin_amdgcn_s_setprio(3);
  else __builtin_amdgcn_s_setprio(POSTPRIO);
  TS(8);
  lds_wait(smem, half ? SY_O0 : SY_Z, 4);
  f32x16 f;
#pragma unroll
  for (int i = 0; i < 4; ++i) {
    if (i == kq) { f = o[i]; continue; }
    char* dst = smem + (i * 3 + (kq < i ? kq : kq - 1)) * 3072 + lane * 16;
#pragma unroll
    for (int g = 0; g < 3; ++g) {
      const f32x4 v = {o[i][4 * g], o[i][4 * g + 1], o[i][4 * g + 2], o[i][4 * g + 3]};
      *(f32x4*)(dst + g * 1024) = v;
    }
  }
  u32x4 zv[3];
  if (half == 0) {
    lds_arrive(smem, SY_R0, lane);
    wait_flags(ws, WS_FZ + (qblk * 32 + (lane & 31)) * FUST, tag);
#pragma unroll
    for (int i = 0; i < 3; ++i) {
      const int n = (i < 2 || wave < 2) ? i * 256 + tid : tid;
      zv[i] = ld16(srd, WS_ZT + (qblk * 80 + (n >> 3)) * 4096 + t0 * 2 + (n & 7) * 16);
    }
    lds_wait(smem, SY_R0, 4);
  } else {
    RAW_BARRIER();
  }
#pragma unroll
  for (int g = 0; g < 9; ++g) {
    const f32x4 v = *(const f32x4*)(smem + kq * 9216 + g * 1024 + lane * 16);
    const int k = 4 * (g % 3);
    f[k] += v[0]; f[k + 1] += v[1]; f[k + 2] += v[2]; f[k + 3] += v[3];
  }
  {
    float big = fabsf(f[0]);
#pragma unroll
    for (int j = 1; j < 12; ++j) big += fabsf(f[j]);
    const bool ok = big < 0x1p126f && (hh == 0 || (f[8] > 0x1p-80f && f[8] < 0x1p80f));
    if (__any(!ok)) attn_safe(srd, vk1, vk2, vv0 + 640, vq1 + kq * 1024, vq2 + kq * 1024, hh, f);
  }
  {
    const float lf = f[8];
    const unsigned lu = __builtin_bit_cast(unsigned, lf);
    const float l = __builtin_bit_cast(float, (unsigned)__builtin_amdgcn_permlane32_swap(lu, lu, false, false)[1]);
    const float inv = __builtin_bit_cast(float, gqu) * __builtin_amdgcn_rcpf(l);
    const unsigned cap = 0xfffffffeu;
    const u32x4 o1 = {min(pk_bf16(f[0] * inv, f[1] * inv), cap), pk_bf16(f[2] * inv, f[3] * inv), min(pk_bf16(f[4] * inv, f[5] * inv), cap),
                      pk_bf16(f[6] * inv, f[7] * inv)};
    const u32x2 o2 = {min(pk_bf16(f[8] * inv, f[9] * inv), cap), pk_bf16(f[10] * inv, f[11] * inv)};
    st16(srd, o_off1, o1);
    st8(srd, o_off2, o2);
  }
  TS(9);
  if (half == 0) {
    __builtin_amdgcn_s_setprio(BTPRIO);
#pragma unroll
    for (int i = 0; i < 3; ++i) {
      const int n = (i < 2 || wave < 2) ? i * 256 + tid : tid;
      *(u32x4*)(smem + L_Z + n * 16) = zv[i];
    }
    WAIT_VM(0);
    if (lds_arrive(smem, SY_O0, lane) == 3 && lane == 0) post_flag(ws, WS_FO + (qblk * 32 + head) * FUST, tag);
    int ln = lane;
    asm volatile("" : "+v"(ln));
    const int r16 = ln & 15, g4 = ln >> 4, q4 = r16 >> 2, p4 = ln & 3;
    unsigned op = (t0 + tc + r16) * 1280 + qblk * 160 + kc * 96 + g4 * 16;
    unsigned wa[6];
    wa[4] = lds_addr(smem) + L_WO + kc * 51200 + (8 * g4 + 4 * (g4 & 1) + q4) * 160 + p4 * 8 + 64;
    wa[5] = wa[4] + ((g4 & 1) ? -640 : 640);
    wa[0] = wa[4] + p4 * 8 + kc * 96 - 64; wa[1] = wa[5] + p4 * 8 + kc * 96 - 64;
    wa[2] = wa[4] + p4 * 8 - kc * 96 + 32; wa[3] = wa[5] + p4 * 8 - kc * 96 + 32;
    unsigned oo = WS_O + ((kc * 40 + g4) * 2048 + t0 + tc + r16) * 16, op2 = kc ? 4u << 20 : op + 64 - g4 * 8;
    unsigned ra = lds_addr(smem) + L_Z + 10240 + (wave & 1) * 5120 + lane * 16;
    asm volatile("" : "+v"(wa[0]), "+v"(wa[1]), "+v"(wa[2]), "+v"(wa[3]), "+v"(wa[4]), "+v"(wa[5]), "+v"(op2), "+v"(oo), "+v"(ra), "+v"(op));
    lds_wait(smem, SY_O0, 4);
    lds_wait(smem, SY_W, 4);
    TS(10);
    f32x4 bt[2][3];
#pragma unroll
    for (int h = 0; h < 2; ++h) {
      const float gi = 1.0f - __builtin_bit_cast(float, giu[h]);
      const char* za = smem + L_Z + q4 * 128 + h * 64 + tc * 2 + p4 * 8;
#pragma unroll
      for (int i = 0; i < 3; ++i) {
        const u32x2 zz = tr_read(za + (i < 2 ? kc * 48 + 8 * g4 + 4 * i : 32 + 4 * g4) * 128);
        const unsigned x0 = i < 2 ? xv[h][2 * (i & 1)] : xw[h][0], x1 = i < 2 ? xv[h][2 * (i & 1) + 1] : xw[h][1];
        const f32x4 v = {gi * bf_lo(x0) * bf_lo(zz[0]), gi * bf_hi(x0) * bf_hi(zz[0]), gi * bf_lo(x1) * bf_lo(zz[1]), gi * bf_hi(x1) * bf_hi(zz[1])};
        const f32x4 zero = {};
        bt[h][i] = i == 2 && kc ? zero : v;
      }
    }
    asm volatile("" : "+v"(bt[0][0]), "+v"(bt[0][1]), "+v"(bt[0][2]), "+v"(bt[1][0]), "+v"(bt[1][1]), "+v"(bt[1][2]));
    const rsrc_t ord = __builtin_amdgcn_make_buffer_rsrc((void*)out, 0, 2048 * 1280, 0x00020000);
    u32x4 wf[8][5], of[10];
    auto rdw = [&](int ks) {
#pragma unroll
      for (int i = 0; i < 5; ++i) {
        const u32x2 w0 = tr_read_a(wa[i & 6] + ks * 5120 + (i & 1) * 8), w1 = tr_read_a(wa[(i & 6) + 1] + ks * 5120 + (i & 1) * 8);
        const u32x4 w = {w0[0], w0[1], w1[0], w1[1]};
        wf[ks][i] = w;
      }
    };
#pragma unroll
    for (int ks = 0; ks < 8; ++ks) rdw(ks);
    SCHED_FENCE();
#pragma unroll
    for (int i = 0; i < 6; ++i) wa[i] += 40960;
    wait_flags(ws, WS_FO + (xcd * 32 + (lane & 31)) * FUST, tag);
    TS(11);
    {
      unsigned so;
      asm volatile(
          "s_mov_b32 %[so], 0\n"
          "buffer_load_dwordx4 v[216:219], %[oo], %[srd], %[so] offen offset:0 sc1\n"
          "s_sleep 2\n"
          "s_mov_b32 %[so], 131072\n"
          "buffer_load_dwordx4 v[220:223], %[oo], %[srd], %[so] offen offset:0 sc1\n"
          "s_sleep 2\n"
          "s_mov_b32 %[so], 262144\n"
          "buffer_load_dwordx4 v[224:227], %[oo], %[srd], %[so] offen offset:0 sc1\n"
          "s_sleep 2\n"
          "s_mov_b32 %[so], 393216\n"
          "buffer_load_dwordx4 v[228:231], %[oo], %[srd], %[so] offen offset:0 sc1\n"
          "s_sleep 2\n"
          "s_mov_b32 %[so], 524288\n"
          "buffer_load_dwordx4 v[232:235], %[oo], %[srd], %[so] offen offset:0 sc1\n"
          "s_sleep 2\n"
          "s_mov_b32 %[so], 655360\n"
          "buffer_load_dwordx4 v[236:239], %[oo], %[srd], %[so] offen offset:0 sc1\n"
          "s_sleep 2\n"
          "s_mov_b32 %[so], 786432\n"
          "buffer_load_dwordx4 v[240:243], %[oo], %[srd], %[so] offen offset:0 sc1\n"
          "s_sleep 2\n"
          "s_mov_b32 %[so], 917504\n"
          "buffer_load_dwordx4 v[244:247], %[oo], %[srd], %[so] offen offset:0 sc1\n"
          "s_sleep 2\n"
          "s_mov_b32 %[so], 1048576\n"
          "buffer_load_dwordx4 v[248:251], %[oo], %[srd], %[so] offen offset:0 sc1\n"
          "s_sleep 2\n"
          "s_mov_b32 %[so], 1179648\n"
          "buffer_load_dwordx4 v[252:255], %[oo], %[srd], %[so] offen offset:0 sc1\n"
          "s_sleep 2\n"
          "s_waitcnt vmcnt(0)\n"
        : "=&{v[216:219]}"(of[0]), "=&{v[220:223]}"(of[1]), "=&{v[224:227]}"(of[2]), "=&{v[228:231]}"(of[3]), "=&{v[232:235]}"(of[4]), "=&{v[236:239]}"(of[5]), "=&{v[240:243]}"(of[6]), "=&{v[244:247]}"(of[7]), "=&{v[248:251]}"(of[8]), "=&{v[252:255]}"(of[9]), [so] "=&s"(so)
        : [oo] "v"(oo), [srd] "s"(srd) : "memory");
    }
    if (b == 0 && wave == 3) {
#pragma unroll 1
      for (int i = 0; i < 4; ++i) wait_flags(ws, WS_FZ + (i * 64 + lane) * FUST, tag);
      if (lane == 0) post_flag(ws, WS_E, tag - 0x9E3779B97F4A7C15ull + 1);
    }
    TS(12);
    __builtin_amdgcn_s_setprio(C1PRIO);
    f32x4 acc[5] = {bt[0][0], bt[0][1], {}, {}, bt[0][2]};
#pragma unroll 1
    for (int h = 0; h < 2; ++h) {
      acc[2] = acc[3] = f32x4{};
      SCHED_FENCE();
#pragma unroll
      for (int ks = 0; ks < 10; ++ks) {
#pragma unroll
        for (int i = 0; i < 5; ++i) acc[i] = mfma16(wf[ks & 7][i], of[ks], acc[i]);
        if (ks < 2) rdw(ks);
        SCHED_FENCE();
      }
      typedef __attribute__((address_space(3))) f32x4* lptr_f32x4;
      *(lptr_f32x4)(size_t)(ra + kc * 2048) = acc[2];
      *(lptr_f32x4)(size_t)(ra + kc * 2048 + 1024) = acc[3];
      if (kc) *(lptr_f32x4)(size_t)(ra + 4096) = acc[4];
      RAW_BARRIER();
      __builtin_amdgcn_s_setprio(2);
      acc[0] += *(lptr_f32x4)(size_t)(ra + (kc ^ 1) * 2048);
      acc[1] += *(lptr_f32x4)(size_t)(ra + (kc ^ 1) * 2048 + 1024);
      acc[4] += *(lptr_f32x4)(size_t)(ra + 4096);
      const u32x4 ov = {pk_bf16(acc[0][0], acc[0][1]), pk_bf16(acc[0][2], acc[0][3]), pk_bf16(acc[1][0], acc[1][1]), pk_bf16(acc[1][2], acc[1][3])};
      __builtin_amdgcn_raw_buffer_store_b128(ov, ord, op, h * 32 * 1280, OUTAUX);
      const u32x2 o2 = {pk_bf16(acc[4][0], acc[4][1]), pk_bf16(acc[4][2], acc[4][3])};
      __builtin_amdgcn_raw_buffer_store_b64(o2, ord, op2, h * 32 * 1280, OUTAUX);
      if (h) break;
      acc[0] = bt[1][0]; acc[1] = bt[1][1]; acc[4] = bt[1][2];
      asm volatile("" : "+v"(acc[0]), "+v"(acc[1]), "+v"(acc[4]));
#pragma unroll
      for (int i = 0; i < 6; ++i) wa[i] -= 40960;
      ra -= 10240;
      if (C1WAIT) __builtin_amdgcn_s_barrier();
      if (PSLEEP0) __builtin_amdgcn_s_sleep(PSLEEP0);
      TS(13);
      {
        unsigned so;
        asm volatile(
          "s_cmp_lg_u32 %[h], 0\n"
          "s_cbranch_scc1 1f\n"
          "s_mov_b32 %[so], 1048576\n"
          "buffer_load_dwordx4 v[216:219], %[oo], %[srd], %[so] offen offset:512 sc1\n"
          "s_mov_b32 %[so], 1179648\n"
          "buffer_load_dwordx4 v[220:223], %[oo], %[srd], %[so] offen offset:512 sc1\n"
          "s_mov_b32 %[so], 262144\n"
          "buffer_load_dwordx4 v[224:227], %[oo], %[srd], %[so] offen offset:512 sc1\n"
          "s_mov_b32 %[so], 393216\n"
          "buffer_load_dwordx4 v[228:231], %[oo], %[srd], %[so] offen offset:512 sc1\n"
          "s_mov_b32 %[so], 524288\n"
          "buffer_load_dwordx4 v[232:235], %[oo], %[srd], %[so] offen offset:512 sc1\n"
          "s_mov_b32 %[so], 655360\n"
          "buffer_load_dwordx4 v[236:239], %[oo], %[srd], %[so] offen offset:512 sc1\n"
          "s_mov_b32 %[so], 786432\n"
          "buffer_load_dwordx4 v[240:243], %[oo], %[srd], %[so] offen offset:512 sc1\n"
          "s_mov_b32 %[so], 917504\n"
          "buffer_load_dwordx4 v[244:247], %[oo], %[srd], %[so] offen offset:512 sc1\n"
          "s_mov_b32 %[so], 0\n"
          "buffer_load_dwordx4 v[248:251], %[oo], %[srd], %[so] offen offset:512 sc1\n"
          "s_mov_b32 %[so], 131072\n"
          "buffer_load_dwordx4 v[252:255], %[oo], %[srd], %[so] offen offset:512 sc1\n"
          "1:\n"
          : "=&{v[216:219]}"(of[0]), "=&{v[220:223]}"(of[1]), "=&{v[224:227]}"(of[2]), "=&{v[228:231]}"(of[3]), "=&{v[232:235]}"(of[4]), "=&{v[236:239]}"(of[5]), "=&{v[240:243]}"(of[6]), "=&{v[244:247]}"(of[7]), "=&{v[248:251]}"(of[8]), "=&{v[252:255]}"(of[9]), [so] "=&s"(so)
          : [oo] "v"(oo), [srd] "s"(srd), [h] "s"(h) : "memory", "scc");
      }
      {
        unsigned left = SPIN_CAP, so, mx; u64 any;
        asm volatile(
          "1:\n"
          "s_waitcnt vmcnt(0)\n"
          "v_max_u32 %[mx], v216, v218\n"
          "v_max3_u32 %[mx], %[mx], v220, v222\n"
          "v_max3_u32 %[mx], %[mx], v224, v226\n"
          "v_max3_u32 %[mx], %[mx], v228, v230\n"
          "v_max3_u32 %[mx], %[mx], v232, v234\n"
          "v_max3_u32 %[mx], %[mx], v236, v238\n"
          "v_max3_u32 %[mx], %[mx], v240, v242\n"
          "v_max3_u32 %[mx], %[mx], v244, v246\n"
          "v_max3_u32 %[mx], %[mx], v248, v250\n"
          "v_max3_u32 %[mx], %[mx], v252, v254\n"
          "v_cmp_eq_u32 vcc, -1, %[mx]\n"
          "s_cbranch_vccz 3f\n"
          "s_sleep %[ps]\n"
          "s_mov_b64 %[any], 0\n"
          "v_max_u32 %[mx], v216, v218\n"
          "s_mov_b32 %[so], 1048576\n"
          "v_cmp_eq_u32 vcc, -1, %[mx]\n"
          "s_cbranch_vccz 2f\n"
          "s_mov_b64 exec, vcc\n"
          "buffer_load_dwordx4 v[216:219], %[oo], %[srd], %[so] offen offset:512 sc1\n"
          "s_mov_b64 exec, -1\n"
          "s_mov_b64 %[any], vcc\n"
          "2:\n"
          "v_max_u32 %[mx], v220, v222\n"
          "s_mov_b32 %[so], 1179648\n"
          "v_cmp_eq_u32 vcc, -1, %[mx]\n"
          "s_cbranch_vccz 2f\n"
          "s_mov_b64 exec, vcc\n"
          "buffer_load_dwordx4 v[220:223], %[oo], %[srd], %[so] offen offset:512 sc1\n"
          "s_mov_b64 exec, -1\n"
          "s_mov_b64 %[any], vcc\n"
          "2:\n"
          "v_max_u32 %[mx], v224, v226\n"
          "s_mov_b32 %[so], 262144\n"
          "v_cmp_eq_u32 vcc, -1, %[mx]\n"
          "s_cbranch_vccz 2f\n"
          "s_mov_b64 exec, vcc\n"
          "buffer_load_dwordx4 v[224:227], %[oo], %[srd], %[so] offen offset:512 sc1\n"
          "s_mov_b64 exec, -1\n"
          "s_mov_b64 %[any], vcc\n"
          "2:\n"
          "v_max_u32 %[mx], v228, v230\n"
          "s_mov_b32 %[so], 393216\n"
          "v_cmp_eq_u32 vcc, -1, %[mx]\n"
          "s_cbranch_vccz 2f\n"
          "s_mov_b64 exec, vcc\n"
          "buffer_load_dwordx4 v[228:231], %[oo], %[srd], %[so] offen offset:512 sc1\n"
          "s_mov_b64 exec, -1\n"
          "s_mov_b64 %[any], vcc\n"
          "2:\n"
          "v_max_u32 %[mx], v232, v234\n"
          "s_mov_b32 %[so], 524288\n"
          "v_cmp_eq_u32 vcc, -1, %[mx]\n"
          "s_cbranch_vccz 2f\n"
          "s_mov_b64 exec, vcc\n"
          "buffer_load_dwordx4 v[232:235], %[oo], %[srd], %[so] offen offset:512 sc1\n"
          "s_mov_b64 exec, -1\n"
          "s_mov_b64 %[any], vcc\n"
          "2:\n"
          "v_max_u32 %[mx], v236, v238\n"
          "s_mov_b32 %[so], 655360\n"
          "v_cmp_eq_u32 vcc, -1, %[mx]\n"
          "s_cbranch_vccz 2f\n"
          "s_mov_b64 exec, vcc\n"
          "buffer_load_dwordx4 v[236:239], %[oo], %[srd], %[so] offen offset:512 sc1\n"
          "s_mov_b64 exec, -1\n"
          "s_mov_b64 %[any], vcc\n"
          "2:\n"
          "v_max_u32 %[mx], v240, v242\n"
          "s_mov_b32 %[so], 786432\n"
          "v_cmp_eq_u32 vcc, -1, %[mx]\n"
          "s_cbranch_vccz 2f\n"
          "s_mov_b64 exec, vcc\n"
          "buffer_load_dwordx4 v[240:243], %[oo], %[srd], %[so] offen offset:512 sc1\n"
          "s_mov_b64 exec, -1\n"
          "s_mov_b64 %[any], vcc\n"
          "2:\n"
          "v_max_u32 %[mx], v244, v246\n"
          "s_mov_b32 %[so], 917504\n"
          "v_cmp_eq_u32 vcc, -1, %[mx]\n"
          "s_cbranch_vccz 2f\n"
          "s_mov_b64 exec, vcc\n"
          "buffer_load_dwordx4 v[244:247], %[oo], %[srd], %[so] offen offset:512 sc1\n"
          "s_mov_b64 exec, -1\n"
          "s_mov_b64 %[any], vcc\n"
          "2:\n"
          "v_max_u32 %[mx], v248, v250\n"
          "s_mov_b32 %[so], 0\n"
          "v_cmp_eq_u32 vcc, -1, %[mx]\n"
          "s_cbranch_vccz 2f\n"
          "s_mov_b64 exec, vcc\n"
          "buffer_load_dwordx4 v[248:251], %[oo], %[srd], %[so] offen offset:512 sc1\n"
          "s_mov_b64 exec, -1\n"
          "s_mov_b64 %[any], vcc\n"
          "2:\n"
          "v_max_u32 %[mx], v252, v254\n"
          "s_mov_b32 %[so], 131072\n"
          "v_cmp_eq_u32 vcc, -1, %[mx]\n"
          "s_cbranch_vccz 2f\n"
          "s_mov_b64 exec, vcc\n"
          "buffer_load_dwordx4 v[252:255], %[oo], %[srd], %[so] offen offset:512 sc1\n"
          "s_mov_b64 exec, -1\n"
          "s_mov_b64 %[any], vcc\n"
          "2:\n"
          "s_cmp_eq_u64 %[any], 0\n"
          "s_cbranch_scc1 3f\n"
          "s_sub_u32 %[left], %[left], 1\n"
          "s_cmp_lg_u32 %[left], 0\n"
          "s_cbranch_scc1 1b\n"
          "s_waitcnt vmcnt(0)\n"
          "3:\n"
          : "+{v[216:219]}"(of[0]), "+{v[220:223]}"(of[1]), "+{v[224:227]}"(of[2]), "+{v[228:231]}"(of[3]), "+{v[232:235]}"(of[4]), "+{v[236:239]}"(of[5]), "+{v[240:243]}"(of[6]), "+{v[244:247]}"(of[7]), "+{v[248:251]}"(of[8]), "+{v[252:255]}"(of[9]),
            [left] "+s"(left), [so] "=&s"(so), [any] "=&s"(any), [mx] "=&v"(mx)
          : [oo] "v"(oo), [srd] "s"(srd), [ps] "n"(PSLEEP1) : "memory", "scc", "vcc");
      }
      TS(14);
    }
    asm volatile("fused_code_end:");
  }
  TS(15);
  TS_FLUSH();
}

extern "C" void kernel_launch(void* const* d_in, const int* in_sizes, int n_in, void* d_out, int out_size, void* d_ws, size_t ws_size,
                              hipStream_t stream) {
  fused<<<256, 512, 0, stream>>>((const char*)d_in[0], (const char*)d_in[1], (const char*)d_in[2], (const char*)d_in[4], (const char*)d_in[5],
                                 (const char*)d_in[6], (const char*)d_in[7], (const char*)d_in[8], (const char*)d_in[9], (char*)d_ws, (char*)d_out);
}
```
